```python
import torch

def _softmax(x, axis=-1):
    return torch.softmax(x, dim=axis)
def _gelu(x):
    return torch.nn.functional.gelu(x, approximate="tanh")
def _silu(x):
    return torch.nn.functional.silu(x)
def _sigmoid(x):
    return torch.sigmoid(x)
def _layernorm(x, axis=-1, eps=1e-5):
    m = x.mean(dim=axis, keepdim=True)
    v = x.var(dim=axis, keepdim=True, unbiased=False)
    return (x - m) / torch.sqrt(v + eps)
def _scatter_add(tgt, idx, src, axis=0):
    return tgt.clone().index_add_(axis, idx, src)
def _segment_sum(src, seg, n):
    out = torch.zeros((n,) + src.shape[1:], dtype=src.dtype)
    return out.index_add_(0, seg, src)
def _pad(x, pw):
    flat = tuple(v for pair in reversed(pw) for v in pair)
    return torch.nn.functional.pad(x, flat)

kernel_name = 'parallel_sum__ssm_conv_s4__moe_arch_mod__prompt'

def setup_inputs():

    _w = (lambda a: (lambda t: t.to(torch.bfloat16) if t.is_floating_point() else t)(torch.as_tensor(a)))
    def _A_setup():
        B, L, D, N = 1, 2048, 512, 128
        A_re = -(lambda _x: torch.exp(torch.as_tensor(_x)))((lambda *s: torch.randn(*s, dtype=torch.bfloat16) * 3.2)(D, N))
        A_im = torch.pi * (lambda *s: torch.randn(*s, dtype=torch.bfloat16) * 0.1)(D, N)
        B_re = (lambda *s: torch.randn(*s, dtype=torch.bfloat16) * 0.4)(D, N)
        B_im = (lambda *s: torch.randn(*s, dtype=torch.bfloat16) * 0.4)(D, N)
        C_re = (lambda *s: torch.randn(*s, dtype=torch.bfloat16) * 0.4)(D, N)
        C_im = (lambda *s: torch.randn(*s, dtype=torch.bfloat16) * 0.4)(D, N)
        log_dt = (lambda *s: torch.randn(*s, dtype=torch.bfloat16) * 0.1)(D) * 0.5 - 4.0
        Dskip = (lambda *s: torch.randn(*s, dtype=torch.bfloat16) * 0.1)(D)
        return {
            "u": _w((lambda *s: torch.randn(*s, dtype=torch.bfloat16) * 0.05)(B, L, D)),
            "A_re": _w(A_re),
            "A_im": _w(A_im),
            "B_re": _w(B_re),
            "B_im": _w(B_im),
            "C_re": _w(C_re),
            "C_im": _w(C_im),
            "log_dt": _w(log_dt),
            "Dskip": _w(Dskip),
        }
    _A_in = _A_setup()

    def _B_setup():
        B, S, D, F, C = 1, 2048, 512, 1792, 2
        return {
            "x": _w((lambda *s: torch.randn(*s, dtype=torch.bfloat16) * 0.05)(B, S, D)),
            "w_r": _w((lambda *s: torch.randn(*s, dtype=torch.bfloat16) * 0.05)(D)),
            "w_gate": _w((lambda *s: torch.randn(*s, dtype=torch.bfloat16) * 0.05)(D, F)),
            "w_up": _w((lambda *s: torch.randn(*s, dtype=torch.bfloat16) * 0.05)(D, F)),
            "w_down": _w((lambda *s: torch.randn(*s, dtype=torch.bfloat16) * 0.05)(F, D)),
        }
    _B_in = _B_setup()

    return {
        "A_u": _A_in["u"],
        "A_A_re": _A_in["A_re"],
        "A_A_im": _A_in["A_im"],
        "A_B_re": _A_in["B_re"],
        "A_B_im": _A_in["B_im"],
        "A_C_re": _A_in["C_re"],
        "A_C_im": _A_in["C_im"],
        "A_log_dt": _A_in["log_dt"],
        "A_Dskip": _A_in["Dskip"],
        "B_x": _B_in["x"],
        "B_w_r": _B_in["w_r"],
        "B_w_gate": _B_in["w_gate"],
        "B_w_up": _B_in["w_up"],
        "B_w_down": _B_in["w_down"],
    }

def reference(A_u, A_A_re, A_A_im, A_B_re, A_B_im, A_C_re, A_C_im, A_log_dt, A_Dskip, B_x, B_w_r, B_w_gate, B_w_up, B_w_down):
    def _A_ref(u, A_re, A_im, B_re, B_im, C_re, C_im, log_dt, Dskip):
        Bsz, L, D = u.shape
        dt = torch.exp(log_dt)[:, None]
        A = A_re + 1j * A_im
        Bc = B_re + 1j * B_im
        Cc = C_re + 1j * C_im
        logdA = dt * A
        dB = dt * Bc
        l = (lambda x, dt: x.to(dt))(torch.arange(L), A_re.dtype)
        P = torch.exp(logdA[:, :, None] * l[None, None, :])
        K = torch.einsum("dn,dnl,dn->dl", Cc, P, dB).real
        L2 = 2 * L
        Kp = _pad(K, ((0, 0), (0, L)))
        up = (lambda x, *d: x.permute(*d))(u, 0, 2, 1)
        up = _pad(up, ((0, 0), (0, 0), (0, L)))
        Kf = (lambda x, axes=(-2,-1): torch.fft.rfft2(x.float(), dim=axes))(Kp[:, None, :], axes=(-2, -1))
        Uf = (lambda x, axes=(-2,-1): torch.fft.rfft2(x.float(), dim=axes))(up[:, :, None, :], axes=(-2, -1))
        Yf = Uf * Kf[None, :, :, :]
        y = (lambda x, s, axes=(-2,-1): torch.fft.irfft2(x, s=s, dim=axes).to(torch.bfloat16))(Yf, (1, L2), axes=(-2, -1))[:, :, 0, :L]
        y = (lambda x, *d: x.permute(*d))(y, 0, 2, 1) + u * Dskip[None, None, :]
        return y

    def _B_ref(x, w_r, w_gate, w_up, w_down):
        B, S, D = x.shape
        C = 2
        r = x @ w_r
        idx = torch.argsort(-r, axis=-1)[:, :C]
        idx_e = (lambda x, n, axis: torch.repeat_interleave(x, n, dim=axis))(idx.reshape(B, C, 1), D, axis=2)
        x_sel = (lambda x, i, axis: torch.take_along_dim(x, i, dim=axis))(x, idx_e, axis=1)
        g = _silu(x_sel @ w_gate)
        u = x_sel @ w_up
        y = (g * u) @ w_down
        r_sel = _sigmoid((lambda x, i, axis: torch.take_along_dim(x, i, dim=axis))(r, idx, axis=1))
        y = y * r_sel.reshape(B, C, 1)
        b_off = (torch.arange(B) * S).reshape(B, 1)
        flat_idx = (idx + b_off).reshape(B * C)
        zero = (x * 0.0).reshape(B * S, D)
        upd = _scatter_add(zero, flat_idx, y.reshape(B * C, D), axis=0)
        return x + upd.reshape(B, S, D)

    x = A_u
    xn = _layernorm(x, axis=-1)
    return x + _A_ref(xn, A_A_re, A_A_im, A_B_re, A_B_im, A_C_re, A_C_im, A_log_dt, A_Dskip) + _B_ref(xn, B_w_r, B_w_gate, B_w_up, B_w_down)
```

```cpp
#include <hip/hip_runtime.h>
#include <hip/hip_bf16.h>
#include <stdint.h>

typedef unsigned short u16;
#define L_ 2048
#define D_ 512
#define N_ 128
#define F_ 1792

__device__ __forceinline__ float bf2f(u16 v) { return __uint_as_float(((unsigned)v) << 16); }
__device__ __forceinline__ u16 f2bf(float f) {
  unsigned u = __float_as_uint(f);
  if ((u & 0x7fffffffu) > 0x7f800000u) return (u16)((u >> 16) | 0x40);
  return (u16)((u + 0x7fffu + ((u >> 16) & 1)) >> 16);
}
__device__ __forceinline__ float rbf(float f) { return bf2f(f2bf(f)); }

__global__ void ln_kernel(const u16* x, const u16* w_r, u16* xn, float* r) {
  int row = blockIdx.x * 4 + (threadIdx.x >> 6);
  int lane = threadIdx.x & 63;
  float v[8];
  float s = 0.f;
  for (int i = 0; i < 8; ++i) { v[i] = bf2f(x[row * D_ + lane * 8 + i]); s += v[i]; }
  for (int o = 32; o; o >>= 1) s += __shfl_xor(s, o);
  float mean = s * (1.f / D_);
  float q = 0.f;
  for (int i = 0; i < 8; ++i) { float d = v[i] - mean; q += d * d; }
  for (int o = 32; o; o >>= 1) q += __shfl_xor(q, o);
  float var = rbf(q * (1.f / D_));
  float m = rbf(mean);
  float sd = rbf(sqrtf(rbf(var + 1e-5f)));
  float acc = 0.f;
  for (int i = 0; i < 8; ++i) {
    float t = rbf(rbf(v[i] - m) / sd);
    xn[row * D_ + lane * 8 + i] = f2bf(t);
    acc += t * bf2f(w_r[lane * 8 + i]);
  }
  for (int o = 32; o; o >>= 1) acc += __shfl_xor(acc, o);
  if (lane == 0) r[row] = rbf(acc);
}

__global__ void kgen_kernel(const u16* A_re, const u16* A_im, const u16* B_re, const u16* B_im,
                            const u16* C_re, const u16* C_im, const u16* log_dt, float* K) {
  int d = blockIdx.y;
  int l = blockIdx.x * 256 + threadIdx.x;
  float dt = rbf(expf(bf2f(log_dt[d])));
  float lb = rbf((float)l);
  float acc = 0.f;
  for (int n = 0; n < N_; ++n) {
    float p = dt * bf2f(A_re[d * N_ + n]), q = dt * bf2f(A_im[d * N_ + n]);
    float br = dt * bf2f(B_re[d * N_ + n]), bi = dt * bf2f(B_im[d * N_ + n]);
    float cr = bf2f(C_re[d * N_ + n]), ci = bf2f(C_im[d * N_ + n]);
    float wr = cr * br - ci * bi, wi = cr * bi + ci * br;
    float e = expf(p * lb);
    float sn, cs;
    sincosf(q * lb, &sn, &cs);
    acc += e * (wr * cs - wi * sn);
  }
  K[d * L_ + l] = acc;
}

__global__ void top2_ffn_kernel(const float* r, const u16* xn, const u16* wg, const u16* wu, const u16* wd,
                                int* idx_out, float* upd) {
  __shared__ int sidx[2];
  __shared__ float xs[2][D_];
  __shared__ float h[2][F_];
  if (threadIdx.x == 0) {
    int i1 = -1, i2 = -1; float v1 = -INFINITY, v2 = -INFINITY;
    for (int i = 0; i < L_; ++i) {
      float v = r[i];
      if (i1 < 0 || v > v1) { v2 = v1; i2 = i1; v1 = v; i1 = i; }
      else if (i2 < 0 || v > v2) { v2 = v; i2 = i; }
    }
    sidx[0] = i1; sidx[1] = i2; idx_out[0] = i1; idx_out[1] = i2;
  }
  __syncthreads();
  for (int i = threadIdx.x; i < 2 * D_; i += blockDim.x) xs[i / D_][i % D_] = bf2f(xn[sidx[i / D_] * D_ + (i % D_)]);
  __syncthreads();
  for (int i = threadIdx.x; i < 2 * F_; i += blockDim.x) {
    int t = i / F_, f = i % F_;
    float g = 0.f, u = 0.f;
    for (int d = 0; d < D_; ++d) { g += xs[t][d] * bf2f(wg[d * F_ + f]); u += xs[t][d] * bf2f(wu[d * F_ + f]); }
    g = rbf(g); u = rbf(u);
    g = rbf(g / (1.f + expf(-g)));
    h[t][f] = rbf(g * u);
  }
  __syncthreads();
  for (int i = threadIdx.x; i < 2 * D_; i += blockDim.x) {
    int t = i / D_, d = i % D_;
    float y = 0.f;
    for (int f = 0; f < F_; ++f) y += h[t][f] * bf2f(wd[f * D_ + d]);
    y = rbf(y);
    float rs = rbf(1.f / (1.f + expf(-r[sidx[t]])));
    upd[i] = rbf(y * rs);
  }
}

__global__ void conv_kernel(const u16* x, const u16* xn, const float* K, const u16* Dskip, const int* idx,
                            const float* upd, u16* out) {
  int d = blockIdx.x * 64 + (threadIdx.x & 63);
  int l = blockIdx.y * 4 + (threadIdx.x >> 6);
  float acc = 0.f;
  for (int j = 0; j <= l; ++j) acc += K[d * L_ + j] * bf2f(xn[(l - j) * D_ + d]);
  float y = rbf(acc);
  float u = bf2f(xn[l * D_ + d]);
  float a = rbf(y + rbf(u * bf2f(Dskip[d])));
  float up = 0.f;
  if (l == idx[0]) up = upd[d];
  if (l == idx[1]) up = upd[D_ + d];
  float b = rbf(u + up);
  out[l * D_ + d] = f2bf(rbf(bf2f(x[l * D_ + d]) + a) + b);
}

extern "C" void kernel_launch(void* const* d_in, const int* in_sizes, int n_in,
                              void* d_out, int out_size, void* d_ws, size_t ws_size,
                              hipStream_t stream) {
  const u16* x = (const u16*)d_in[0];
  char* ws = (char*)d_ws;
  u16* xn = (u16*)ws;
  float* r = (float*)(ws + (2 << 20));
  int* idx = (int*)(ws + (2 << 20) + 16384);
  float* upd = (float*)(ws + (2 << 20) + 32768);
  float* K = (float*)(ws + (4 << 20));
  ln_kernel<<<L_ / 4, 256, 0, stream>>>(x, (const u16*)d_in[10], xn, r);
  kgen_kernel<<<dim3(L_ / 256, D_), 256, 0, stream>>>((const u16*)d_in[1], (const u16*)d_in[2], (const u16*)d_in[3],
                                                     (const u16*)d_in[4], (const u16*)d_in[5], (const u16*)d_in[6],
                                                     (const u16*)d_in[7], K);
  top2_ffn_kernel<<<1, 1024, 0, stream>>>(r, xn, (const u16*)d_in[11], (const u16*)d_in[12], (const u16*)d_in[13], idx, upd);
  conv_kernel<<<dim3(D_ / 64, L_ / 4), 256, 0, stream>>>(x, xn, K, (const u16*)d_in[8], idx, upd, (u16*)d_out);
}
```

```cpp
#include <hip/hip_runtime.h>
#include <stdint.h>

typedef unsigned short u16;
typedef unsigned int u32;
typedef unsigned long long u64;
typedef u32 u32x2 __attribute__((ext_vector_type(2)));
typedef u32 u32x4 __attribute__((ext_vector_type(4)));
typedef float f32x4 __attribute__((ext_vector_type(4)));
typedef float f32x2 __attribute__((ext_vector_type(2)));
typedef _Float16 h2 __attribute__((ext_vector_type(2)));
typedef _Float16 h8 __attribute__((ext_vector_type(8)));
typedef __bf16 b2 __attribute__((ext_vector_type(2)));
typedef __bf16 b8 __attribute__((ext_vector_type(8)));
typedef short s4 __attribute__((ext_vector_type(4)));
#define LAS __attribute__((address_space(3)))
#define DEV __device__ __forceinline__

#define WS_CTRL 0
#define WS_STATS 4096
#define WS_KEYS 32768
#define WS_ACKS 65536
#define WS_PART 131072
#define WS_BYTES (2u << 20)
#define NPROD 112
#define SPIN_CAP 100000

#define TAB_RS 2080
#define KL_BASE 112320
#define KL_CS 1984
#define KL_CH (4 * KL_CS)
#define XO_BASE (KL_BASE + 4 * KL_CH)
#define MISC_BASE (XO_BASE + 16384)
#define LDS_BYTES (MISC_BASE + 256)
#define DCH 14944
#define U_OFF 480
#define W5_OFF (4576 + 32)
#define W3_OFF (W5_OFF + 3104)
#define W9_OFF (W3_OFF + 3104)
#define W7_OFF (W9_OFF + 2080)
#define KG0 592
#define KGS 52
#define KG4 (KG0 + 4 * KGS)
#define KG5 (KG4 + 84)

#define STAMP(i)

DEV float bflo(u32 v) { return __uint_as_float(v << 16); }
DEV float bfhi(u32 v) { return __uint_as_float(v & 0xffff0000u); }
DEV u32 pkbf(float a, float b) { f32x2 f = {a, b}; return __builtin_bit_cast(u32, __builtin_convertvector(f, b2)); }
DEV float rbf(float a) { return bflo(pkbf(a, a)); }
DEV u32 pkh(float a, float b) { h2 h = {(_Float16)a, (_Float16)b}; return __builtin_bit_cast(u32, h); }
DEV u32 hadd(u32 a, u32 b) { return __builtin_bit_cast(u32, __builtin_bit_cast(h2, a) + __builtin_bit_cast(h2, b)); }
DEV f32x4 mma_h(u32x4 a, u32x4 b, f32x4 c) {
  return __builtin_amdgcn_mfma_f32_16x16x32_f16(__builtin_bit_cast(h8, a), __builtin_bit_cast(h8, b), c, 0, 0, 0);
}
DEV f32x4 mma_b(u32x4 a, u32x4 b, f32x4 c) {
  return __builtin_amdgcn_mfma_f32_16x16x32_bf16(__builtin_bit_cast(b8, a), __builtin_bit_cast(b8, b), c, 0, 0, 0);
}
template <int C> DEV u32 dppu(u32 v) { return __builtin_amdgcn_update_dpp(0, v, C, 0xf, 0xf, true); }
template <int C> DEV float dppf(float v) { return __uint_as_float(dppu<C>(__float_as_uint(v))); }
#define ROR(n) (0x120 + (n))
DEV float sum16(float v) { u32 u = __float_as_uint(v); auto r = __builtin_amdgcn_permlane16_swap(u, u, false, false); return __uint_as_float(r[0]) + __uint_as_float(r[1]); }
DEV float sum32(float v) { u32 u = __float_as_uint(v); auto r = __builtin_amdgcn_permlane32_swap(u, u, false, false); return __uint_as_float(r[0]) + __uint_as_float(r[1]); }
DEV u32 rowmax(u32 v) {
  v = max(v, dppu<ROR(1)>(v)); v = max(v, dppu<ROR(2)>(v)); v = max(v, dppu<ROR(4)>(v)); v = max(v, dppu<ROR(8)>(v));
  return v;
}
DEV u32 wavemax(u32 v) {
  v = rowmax(v);
  u32 a = __builtin_amdgcn_readlane(v, 0), b = __builtin_amdgcn_readlane(v, 16), c = __builtin_amdgcn_readlane(v, 32), d = __builtin_amdgcn_readlane(v, 48);
  return max(max(a, b), max(c, d));
}
DEV void bar() { asm volatile("s_waitcnt lgkmcnt(0)\n\ts_barrier" ::: "memory"); }

struct cf { float r, i; };
DEV cf cmul(cf a, cf b) { cf o; o.r = __builtin_fmaf(a.r, b.r, -(a.i * b.i)); o.i = __builtin_fmaf(a.r, b.i, a.i * b.r); return o; }
DEV cf csq(cf a) { cf o; o.r = __builtin_fmaf(a.r, a.r, -(a.i * a.i)); o.i = (a.r + a.r) * a.i; return o; }
DEV cf seed(float p2, float th, float tl, float v) {
  float e = __builtin_amdgcn_exp2f(p2 * v);
  float f = __builtin_fmaf(tl, v, __builtin_amdgcn_fractf(th * v));
  cf o; o.r = e * __builtin_amdgcn_cosf(f); o.i = e * __builtin_amdgcn_sinf(f); return o;
}

template <typename T> DEV T lds_ld(LAS char* s, u32 off) { return *(LAS T*)(s + off); }
template <typename T> DEV void lds_st(LAS char* s, u32 off, T v) { *(LAS T*)(s + off) = v; }
DEV u32x2 lds_tr(LAS char* s, u32 off) { return __builtin_bit_cast(u32x2, __builtin_amdgcn_ds_read_tr16_b64_v4i16((LAS s4*)(s + off))); }
DEV u32x4 lds_tr2(LAS char* s, u32 o0, u32 o1) { u32x2 a = lds_tr(s, o0), b = lds_tr(s, o1); u32x4 r = {a.x, a.y, b.x, b.y}; return r; }

DEV void top2(__amdgpu_buffer_rsrc_t wr, u32 tag, int lane, u32x4 (&kk)[4], u32& K1, u32& K2) {
  for (int spin = 0; spin < SPIN_CAP; ++spin) {
    bool ok = true;
#pragma unroll
    for (int j = 0; j < 4; ++j) {
      kk[j] = __builtin_amdgcn_raw_buffer_load_b128(wr, WS_KEYS + (lane >> 1) * 64 + (lane & 1) * 16 + j * 2048, 0, 16);
      ok &= (kk[j].y == tag) & (kk[j].w == tag);
    }
    if (__all(ok)) break;
    __builtin_amdgcn_s_sleep(1);
  }
  K1 = wavemax(max(max(kk[0].x, kk[1].x), max(kk[2].x, kk[3].x)));
  u32 m = 0;
#pragma unroll
  for (int j = 0; j < 4; ++j) m = max(m, kk[j].x == K1 ? 0u : kk[j].x);
  K2 = wavemax(m);
}

struct KFr { u32x4 a[18], s5[3], s3[3], t[4]; };

template <int T> DEV f32x4 far_taps(LAS char* sm, const KFr& K, u32 dch, u32 ub, u32 w2b, u32 w3b, f32x4 acc) {
#pragma unroll
  for (int i = 0; i < 3; ++i) {
    const int u = T + i - 4;
    if (u >= 0) {
      acc = mma_h(lds_tr2(sm, w2b + W5_OFF + 512 * u, w2b + W5_OFF + 512 * u + 128), K.s5[i], acc);
      acc = mma_h(lds_tr2(sm, w2b + W3_OFF + 8 + 512 * u, w2b + W3_OFF + 8 + 512 * u + 128), K.s3[i], acc);
    }
  }
  if (T >= 4) {
#pragma unroll
    for (int i = 0; i <= T - 4; ++i) {
      const int u = T - 4 - i;
      acc = mma_h(lds_tr2(sm, w3b + 512 * u, w3b + 512 * u + 256), K.t[i], acc);
    }
    acc = mma_h(K.a[17], lds_ld<u32x4>(sm, ub + 2 * (256 * T - 1032 + 16)), acc);
  }
  return acc;
}

__global__ __launch_bounds__(512, 2) void fused_kernel(
    const u16* __restrict__ x, const u16* __restrict__ pAre, const u16* __restrict__ pAim, const u16* __restrict__ pBre,
    const u16* __restrict__ pBim, const u16* __restrict__ pCre, const u16* __restrict__ pCim, const u16* __restrict__ pLdt,
    const u16* __restrict__ pDs, const u16* __restrict__ pWr, const u16* __restrict__ pWg, const u16* __restrict__ pWu,
    const u16* __restrict__ pWd, u16* __restrict__ out, char* ws) {
  __shared__ __attribute__((aligned(16))) char smem[LDS_BYTES];
  LAS char* sm = (LAS char*)smem;
  const int tid = threadIdx.x, lane = tid & 63;
  const int w = __builtin_amdgcn_readfirstlane(tid >> 6);
  const int c = lane & 15, g = lane >> 4, q = (lane >> 2) & 3, p = lane & 3;
  const int b = blockIdx.x;
  const __amdgpu_buffer_rsrc_t wr = __builtin_amdgcn_make_buffer_rsrc(ws, 0, WS_BYTES, 0x00020000);
  STAMP(0);

  if (b < 128) {
    const u16* xr = x + (16 * b + c) * 512 + 64 * w + 16 * g;
    u32x4 x0 = *(const u32x4*)xr, x1 = *(const u32x4*)(xr + 8);
    u32x4 w0 = *(const u32x4*)(pWr + 64 * w + 16 * g), w1 = *(const u32x4*)(pWr + 64 * w + 16 * g + 8);
    const u32 tag = __builtin_amdgcn_raw_buffer_load_b32(wr, WS_CTRL, 0, 16) + 1u;
    u32x4 wa[4], wb[2];
    const int bb = b < NPROD ? b : 0;
    {
      const u16* src = (w & 4 ? pWu : pWg) + (128 * (w & 3) + (lane >> 1)) * 1792 + 16 * bb + 8 * (lane & 1);
#pragma unroll
      for (int j = 0; j < 4; ++j) wa[j] = *(const u32x4*)(src + j * 32 * 1792);
      const u16* sd = pWd + (16 * bb + (lane >> 3)) * 512 + 64 * w + 8 * (lane & 7);
#pragma unroll
      for (int j = 0; j < 2; ++j) wb[j] = *(const u32x4*)(sd + j * 8 * 512);
    }
    const u32x4 ones = {0x3f803f80u, 0x3f803f80u, 0x3f803f80u, 0x3f803f80u};
    const bool selw = (c & 1) != 0;
    u32x4 a0 = selw ? w0 : ones, a1 = selw ? w1 : ones;
    const f32x4 z4 = {0.f, 0.f, 0.f, 0.f};
    f32x4 d3 = mma_b(a1, ones, mma_b(a0, ones, z4));
    f32x4 d1 = mma_b(a1, x1, mma_b(a0, x0, z4));
    f32x4 d2 = mma_b(x1, x1, mma_b(x0, x0, z4));
    {
      float dg = p == 0 ? d2.x : p == 1 ? d2.y : p == 2 ? d2.z : d2.w;
      f32x4 pr = {d1.x, d1.y, dg, d3.y};
      if (g == q) lds_st<f32x4>(sm, w * 256 + c * 16, pr);
    }
    STAMP(1);
    bar();
    if (w == 0) {
      f32x4 s = lds_ld<f32x4>(sm, g * 512 + c * 16) + lds_ld<f32x4>(sm, g * 512 + 256 + c * 16);
      s.x = sum32(sum16(s.x)); s.y = sum32(sum16(s.y)); s.z = sum32(sum16(s.z)); s.w = sum32(sum16(s.w));
      float mean = s.x * (1.f / 512.f);
      float var = fmaxf(__builtin_fmaf(-mean, mean, s.z * (1.f / 512.f)), 0.f);
      float sd = __builtin_amdgcn_sqrtf(rbf(rbf(var) + 1e-5f));
      u32 ms = pkbf(mean, sd);
      u32 msn = dppu<0xb1>(ms);
      u32x4 gs = {ms, tag, msn, tag};
      if (lane < 16 && !(lane & 1)) __builtin_amdgcn_raw_buffer_store_b128(gs, wr, WS_STATS + b * 128 + lane * 8, 0, 16);
      float r = __builtin_fmaf(-bflo(ms), s.w, s.y) * __builtin_amdgcn_rcpf(bfhi(ms));
      u32 rb = pkbf(r, r) & 0xffff0000u;
      rb ^= ((int)rb < 0) ? 0xffff0000u : 0x80000000u;
      u32 key = rb | (u32)(2048 - (16 * b + c));
      u32 k1 = rowmax(key);
      u32 k2 = rowmax(key == k1 ? 0u : key);
      u32x4 gk = {key, tag, ms, tag};
      if (lane < 16 && key >= k2) __builtin_amdgcn_raw_buffer_store_b128(gk, wr, WS_KEYS + b * 64 + (key == k1 ? 0 : 16), 0, 16);
      STAMP(2);
    }
    if (b >= NPROD) {
      if (b == 127 && w == 0) {
        u32x4 kk[4]; u32 K1, K2;
        top2(wr, tag, lane, kk, K1, K2);
        for (int spin = 0; spin < SPIN_CAP; ++spin) {
          u32x2 a = __builtin_amdgcn_raw_buffer_load_b64(wr, WS_ACKS + lane * 8, 0, 16);
          if (__all(a.x == tag && a.y == tag)) break;
          __builtin_amdgcn_s_sleep(2);
        }
        if (lane == 0) __builtin_amdgcn_raw_buffer_store_b32(tag, wr, WS_CTRL, 0, 16);
      }
      return;
    }
    const u32 wbase = 8192 + w * 6144;
#pragma unroll
    for (int j = 0; j < 4; ++j) lds_st<u32x4>(sm, wbase + j * 1024 + lane * 16, wa[j]);
#pragma unroll
    for (int j = 0; j < 2; ++j) lds_st<u32x4>(sm, wbase + 4096 + j * 1024 + lane * 16, wb[j]);
    u32x4 fa[4]; u32x2 fb[4];
#pragma unroll
    for (int j = 0; j < 4; ++j) {
      fa[j] = lds_tr2(sm, wbase + j * 1024 + (8 * g + q) * 32 + 8 * p, wbase + j * 1024 + (8 * g + q) * 32 + 8 * p + 128);
      fb[j] = lds_tr(sm, wbase + 4096 + (4 * g + q) * 128 + j * 32 + 8 * p);
    }
    STAMP(3);
    if (w == 0) {
      u32x4 kk[4]; u32 K1, K2;
      top2(wr, tag, lane, kk, K1, K2);
#pragma unroll
      for (int j = 0; j < 4; ++j)
        if (kk[j].x >= K2) { u32x2 v = {kk[j].x, kk[j].z}; lds_st<u32x2>(sm, 4096 + (kk[j].x == K1 ? 0 : 8), v); }
    }
    bar();
    STAMP(4);
    {
      u32x2 sel = lds_ld<u32x2>(sm, 4096 + (w >> 2) * 8);
      int row = (2048 - (sel.x & 0xfff)) & 2047;
      u32 xv = *(const u32*)(x + row * 512 + 2 * (tid & 255));
      float m = bflo(sel.y), rs = __builtin_amdgcn_rcpf(bfhi(sel.y));
      u32 t = pkbf(bflo(xv) - m, bfhi(xv) - m);
      lds_st<u32>(sm, 4352 + tid * 4 + (w >> 2) * 16, pkbf(bflo(t) * rs, bfhi(t) * rs));
    }
    bar();
    STAMP(5);
    {
      const u32 xb = 4352 + ((c >> 2) & 1) * 1040 + (w & 3) * 256 + g * 16;
      f32x4 d = z4;
#pragma unroll
      for (int j = 0; j < 4; ++j) d = mma_b(fa[j], lds_ld<u32x4>(sm, xb + j * 64), d);
      if ((c & 3) == (w & 3) && (c >> 3) == (w >> 2)) lds_st<f32x4>(sm, c * 64 + g * 16, d);
    }
    bar();
    {
      f32x4 s = lds_ld<f32x4>(sm, c * 64 + g * 16);
      float hv[4];
#pragma unroll
      for (int i = 0; i < 4; ++i) {
        float v = s[i];
        v += dppf<0xb1>(v);
        v += dppf<0x4e>(v);
        v = rbf(v);
        float o = dppf<ROR(8)>(v);
        float ga = (c & 8) ? o : v, up = (c & 8) ? v : o;
        float sl = ga * __builtin_amdgcn_rcpf(1.f + __builtin_amdgcn_exp2f(ga * -1.44269504f));
        hv[i] = rbf(sl) * up;
      }
      u32x2 h = {pkbf(hv[0], hv[1]), pkbf(hv[2], hv[3])};
      const u32x2 z2 = {0u, 0u};
      f32x4 d = z4;
#pragma unroll
      for (int j = 0; j < 4; ++j)
        d = __builtin_amdgcn_mfma_f32_16x16x16bf16_1k(__builtin_bit_cast(s4, fb[j]), __builtin_bit_cast(s4, (c & 3) == j ? h : z2), d, 0, 0, 0);
      u32x4 gp = {__float_as_uint((c & 8) ? d.z : d.x), tag, __float_as_uint((c & 8) ? d.w : d.y), tag};
      const u32 grp = 16 * w + 4 * (c & 3) + g;
      __builtin_amdgcn_raw_buffer_store_b128(gp, wr, WS_PART + (grp * NPROD + b) * 64 + ((c >> 2) & 1) * 32 + (c >> 3) * 16, 0, 16);
    }
    STAMP(6);
    return;
  }

  const int cb = b - 128;
  const int grp = (cb & 7) * 16 + (cb >> 3);
  const int ch = w & 3, hb = w >> 2;
  const int chan = 4 * grp + ch;
  const u32 vAre = *(const u32*)(pAre + chan * 128 + 2 * lane), vAim = *(const u32*)(pAim + chan * 128 + 2 * lane);
  const u32 vBre = *(const u32*)(pBre + chan * 128 + 2 * lane), vBim = *(const u32*)(pBim + chan * 128 + 2 * lane);
  const u32 vCre = *(const u32*)(pCre + chan * 128 + 2 * lane), vCim = *(const u32*)(pCim + chan * 128 + 2 * lane);
  const u32 vld = pLdt[chan], vds = pDs[chan];
  const u32 tag = __builtin_amdgcn_raw_buffer_load_b32(wr, WS_CTRL, 0, 16) + 1u;
  u32x2 xc[4];
#pragma unroll
  for (int i = 0; i < 4; ++i) xc[i] = *(const u32x2*)(x + (4 * tid + i) * 512 + 4 * grp);
  {
    const u32x4 z = {0u, 0u, 0u, 0u};
    if (tid < 480) lds_st<u32x4>(sm, KL_BASE + (tid / 120) * KL_CH + (tid % 120) * 16, z);
    if (tid >= 496) { const u32x4 mi = {0u, 0u, 0u, 0u}; lds_st<u32x4>(sm, MISC_BASE + (tid - 496) * 16, mi); }
  }
  float inv, dsc;
  {
    const float dt = rbf(__builtin_amdgcn_exp2f(bflo(vld) * 1.44269504f));
    const float d1 = 1.f + bflo(vds);
    const float dl = dt * 1.44269504f;
    float b0 = (fabsf(bflo(vCre)) + fabsf(bflo(vCim))) * (fabsf(bflo(vBre)) + fabsf(bflo(vBim))) * __builtin_amdgcn_exp2f(fmaxf(bflo(vAre), 0.f) * dl * 2048.f);
    float b1 = (fabsf(bfhi(vCre)) + fabsf(bfhi(vCim))) * (fabsf(bfhi(vBre)) + fabsf(bfhi(vBim))) * __builtin_amdgcn_exp2f(fmaxf(bfhi(vAre), 0.f) * dl * 2048.f);
    float bd = fmaxf(fmaxf(b0, b1) * dt, fabsf(d1) * (1.f / 128.f));
    int be = (int)(wavemax(__float_as_uint(bd)) >> 23);
    int se = min(max(260 - be, 1), 253);
    const float sc = __uint_as_float((u32)se << 23);
    inv = __uint_as_float((u32)(254 - se) << 23);
    dsc = d1 * sc;
    const float are = hb ? bfhi(vAre) : bflo(vAre), aim = hb ? bfhi(vAim) : bflo(vAim);
    cf B = {(hb ? bfhi(vBre) : bflo(vBre)) * dt, (hb ? bfhi(vBim) : bflo(vBim)) * dt};
    cf C = {(hb ? bfhi(vCre) : bflo(vCre)) * sc, (hb ? bfhi(vCim) : bflo(vCim)) * sc};
    const float p2 = (dt * are) * 1.44269504f;
    const float qq = dt * aim;
    const float th = qq * 0.15915494f;
    const float tl = __builtin_fmaf(qq, 6.4206382e-9f, __builtin_fmaf(qq, 0.15915494f, -th));
    const u32 tb = ch * 512 + hb * 256 + lane * 4;
#define WQ(bi, v) lds_st<u32>(sm, tb + (((bi) & 1) * 16 + ((bi) >> 1)) * TAB_RS, pkh((v).r, -(v).i))
#define WP(ri, v) lds_st<u32>(sm, tb + (32 + (ri)) * TAB_RS, pkh((v).r, (v).i))
    cf E1 = seed(p2, th, tl, 1.f), E16 = seed(p2, th, tl, 16.f), E256 = seed(p2, th, tl, 256.f);
    cf E2 = csq(E1), E4 = csq(E2), E8 = csq(E4), E32 = csq(E16), E64 = csq(E32), E128 = csq(E64), E512 = csq(E256), E1k = csq(E512);
    cf Q[16];
    Q[0] = cf{1.f, 0.f}; Q[1] = E8; Q[2] = E16; Q[3] = cmul(E8, E16); Q[4] = E32;
#pragma unroll
    for (int i = 5; i < 8; ++i) Q[i] = cmul(Q[i - 4], E32);
    Q[8] = E64;
#pragma unroll
    for (int i = 9; i < 16; ++i) Q[i] = cmul(Q[i - 8], E64);
#pragma unroll
    for (int i = 0; i < 16; ++i) WQ(i, Q[i]);
    WQ(16, E128);
#pragma unroll
    for (int i = 17; i < 32; ++i) { cf t = cmul(Q[i - 16], E128); WQ(i, t); }
    cf P0 = cmul(C, B), P1 = cmul(P0, E1), P2 = cmul(P0, E2), P3 = cmul(P1, E2), P4 = cmul(P0, E4);
    cf P5 = cmul(P1, E4), P6 = cmul(P2, E4), P7 = cmul(P3, E4);
    WP(0, P0); WP(1, P1); WP(2, P2); WP(3, P3); WP(4, P4); WP(5, P5); WP(6, P6); WP(7, P7);
    cf R0 = cmul(P0, E256), R2 = cmul(P2, E256), R4 = cmul(P4, E256), R6 = cmul(P6, E256), R8 = cmul(R0, E8);
    WP(8, R0); WP(9, R2); WP(10, R4); WP(11, R6); WP(12, R8);
    cf S0 = cmul(P0, E512), S1 = cmul(R0, E512), S2 = cmul(P4, E512), S3 = cmul(R4, E512);
    WP(13, S0); WP(14, S1); WP(15, S2); WP(16, S3);
    cf T0 = cmul(P0, E1k), T1 = cmul(R0, E1k), T2 = cmul(S0, E1k), T3 = cmul(S1, E1k), T4 = cmul(T0, E1k);
    WP(17, T0); WP(18, T1); WP(19, T2); WP(20, T3); WP(21, T4);
  }
  if (tid == 0) __builtin_amdgcn_raw_buffer_store_b32(tag, wr, WS_ACKS + cb * 4, 0, 16);
  STAMP(1);
  bar();
  {
    const int ra = c < 8 ? c : (int)((0xcbaaa988u >> ((c - 8) * 4)) & 15);
    const int rb = c < 9 ? 13 + c : (c == 12 ? 13 : 17);
    const u32 qa = ch * 512 + g * 16 + (hb * 16 + c) * TAB_RS;
    const u32 pa = ch * 512 + g * 16 + (32 + ra) * TAB_RS;
    const u32 pb = ch * 512 + g * 16 + (32 + rb) * TAB_RS;
    f32x4 da = {0.f, 0.f, 0.f, 0.f}, db = da;
#pragma unroll
    for (int k = 0; k < 8; ++k) {
      u32x4 fq = lds_ld<u32x4>(sm, qa + k * 64);
      da = mma_h(lds_ld<u32x4>(sm, pa + k * 64), fq, da);
      db = mma_h(fq, lds_ld<u32x4>(sm, pb + k * 64), db);
    }
    if (lane == 0 && hb == 0) da.x += dsc;
    const u32 kl = KL_BASE + ch * KL_CH;
    u32x2 va = {pkh(da.x, da.y), pkh(da.z, da.w)};
    lds_st<u32x2>(sm, kl + 2 * (16 + (g >> 1) * 256 + 16 * c + 8 * hb + 4 * (g & 1)), va);
    u32x2 vb = {pkh(db.x, db.y), pkh(db.z, db.w)};
    const u32 h0 = vb.x & 0xffffu;
    int dst = c < 2 ? KG0 + hb * KGS + 16 * c : c < 4 ? KG0 + (3 - hb) * KGS + 16 * (c - 2) : (hb ? KG5 : KG4) + 16 * (c - 4);
    dst += 4 * g;
    if (hb == 0 && g == 0) {
      if (c == 0 || c == 4) vb.x &= 0xffff0000u;
      if (c >= 8) { vb.x = h0; vb.y = 0u; }
      if (c == 9) dst = KG0 + 32;
      if (c == 10) dst = 572;
      if (c == 11) { dst = 568; vb.x = 0u; vb.y = h0 << 16; }
      if (c == 12) { dst = 528; vb.x = h0 | (h0 << 16); vb.y = h0; }
    }
    if (c < 8 || (hb == 0 && g == 0 && c <= 12)) lds_st<u32x2>(sm, kl + 2 * dst, vb);
  }
  bar();
  {
    const u32 kl = KL_BASE + ch * KL_CH;
#pragma unroll
    for (int it = 0; it < 2; ++it) {
      const int xk = it * 128 + hb * 64 + lane;
      if (xk < 238) {
        u32x2 r0 = lds_ld<u32x2>(sm, kl + xk * 8), r1 = lds_ld<u32x2>(sm, kl + xk * 8 + 8);
        u32 m0 = __builtin_amdgcn_alignbit(r0.y, r0.x, 16), m1 = __builtin_amdgcn_alignbit(r1.x, r0.y, 16), m2 = __builtin_amdgcn_alignbit(r1.y, r1.x, 16);
        u32x2 c1 = {m0, m1}, c2 = {r0.y, r1.x}, c3 = {m1, m2};
        lds_st<u32x2>(sm, kl + KL_CS + xk * 8, c1);
        lds_st<u32x2>(sm, kl + 2 * KL_CS + xk * 8, c2);
        lds_st<u32x2>(sm, kl + 3 * KL_CS + xk * 8, c3);
      }
    }
    const u32x2 z2 = {0u, 0u};
    if (hb == 0) { if (lane < 60) lds_st<u32x2>(sm, ch * DCH + lane * 8, z2); }
    else if (lane < 16) {
      const u32 o = q == 0 ? W5_OFF : q == 1 ? W3_OFF : q == 2 ? W9_OFF : W7_OFF;
      lds_st<u32x2>(sm, ch * DCH + o - 32 + p * 8, z2);
    }
  }
  bar();
  STAMP(2);
  KFr K;
  {
    const u32 kl = KL_BASE + ch * KL_CH + 8 * p;
    {
      const int io = 32 - 8 * g - q - 4, sg = io & 3;
      const u32 a = kl + sg * KL_CS + 2 * (io - sg);
#pragma unroll
      for (int t = 0; t < 18; ++t) K.a[t] = lds_tr2(sm, a + 64 * t + 8, a + 64 * t);
    }
    {
      const int kap = 16 * (g >> 1) + 4 * (g & 1) + q + 8;
      const int n0 = 130 - 64 - kap, par = n0 & 1, a0 = n0 >> 1;
      const int i5 = KG0 + par * KGS + a0 - 32, i3 = par ? KG0 + 3 * KGS + a0 - 32 : KG0 + 2 * KGS + a0 - 33;
      const u32 a5 = kl + (i5 & 3) * KL_CS + 2 * (i5 & ~3), a3 = kl + (i3 & 3) * KL_CS + 2 * (i3 & ~3);
#pragma unroll
      for (int i = 0; i < 3; ++i) {
        K.s5[i] = lds_tr2(sm, a5 + 32 * (2 - i) + 8, a5 + 32 * (2 - i));
        K.s3[i] = lds_tr2(sm, a3 + 32 * (2 - i) + 8, a3 + 32 * (2 - i));
      }
    }
    {
      const int n0 = 65 - (8 + 4 * (g & 1) + q);
      const int it = g < 2 ? KG4 + n0 - 64 : KG5 + n0 - 65;
      const u32 a = kl + (it & 3) * KL_CS + 2 * (it & ~3);
#pragma unroll
      for (int i = 0; i < 4; ++i) K.t[i] = lds_tr2(sm, a + 32 * i + 16, a + 32 * i);
    }
  }
  u32x2 xt[4];
  xt[0] = u32x2{__builtin_amdgcn_perm(xc[1].x, xc[0].x, 0x05040100), __builtin_amdgcn_perm(xc[3].x, xc[2].x, 0x05040100)};
  xt[1] = u32x2{__builtin_amdgcn_perm(xc[1].x, xc[0].x, 0x07060302), __builtin_amdgcn_perm(xc[3].x, xc[2].x, 0x07060302)};
  xt[2] = u32x2{__builtin_amdgcn_perm(xc[1].y, xc[0].y, 0x05040100), __builtin_amdgcn_perm(xc[3].y, xc[2].y, 0x05040100)};
  xt[3] = u32x2{__builtin_amdgcn_perm(xc[1].y, xc[0].y, 0x07060302), __builtin_amdgcn_perm(xc[3].y, xc[2].y, 0x07060302)};
#pragma unroll
  for (int i = 0; i < 4; ++i) lds_st<u32x2>(sm, XO_BASE + i * 4096 + tid * 8, xt[i]);
  STAMP(3);
  {
    u32x4 s0, s1;
    for (int spin = 0; spin < SPIN_CAP; ++spin) {
      s0 = __builtin_amdgcn_raw_buffer_load_b128(wr, WS_STATS + tid * 32, 0, 16);
      s1 = __builtin_amdgcn_raw_buffer_load_b128(wr, WS_STATS + tid * 32 + 16, 0, 16);
      if (__all((s0.y == tag) & (s0.w == tag) & (s1.y == tag) & (s1.w == tag))) break;
      __builtin_amdgcn_s_sleep(1);
    }
    STAMP(4);
    const float r0 = __builtin_amdgcn_rcpf(bfhi(s0.x)), r1 = __builtin_amdgcn_rcpf(bfhi(s0.z));
    const float r2 = __builtin_amdgcn_rcpf(bfhi(s1.x)), r3 = __builtin_amdgcn_rcpf(bfhi(s1.z));
    const float n0 = -bflo(s0.x) * r0, n1 = -bflo(s0.z) * r1, n2 = -bflo(s1.x) * r2, n3 = -bflo(s1.z) * r3;
#pragma unroll
    for (int i = 0; i < 4; ++i) {
      u32x2 u = {pkh(__builtin_fmaf(bflo(xt[i].x), r0, n0), __builtin_fmaf(bfhi(xt[i].x), r1, n1)),
                 pkh(__builtin_fmaf(bflo(xt[i].y), r2, n2), __builtin_fmaf(bfhi(xt[i].y), r3, n3))};
      lds_st<u32x2>(sm, i * DCH + U_OFF + tid * 8, u);
    }
  }
  bar();
  STAMP(5);
  const u32 dch = ch * DCH;
  const u32 ub = dch + U_OFF - 32 + 32 * c + 16 * g;
  const u32 w2b = dch - 32 + (16 * (g >> 1) + 4 * (g & 1) + q) * 16 + 8 * p;
  const u32 w3b = dch + (g < 2 ? W9_OFF : W7_OFF + 16) - 32 + (4 * (g & 1) + q) * 32 + 8 * p;
  const u32 xo = XO_BASE + ch * 4096 + 32 * c + 8 * g;
  volatile LAS u32* misc = (volatile LAS u32*)(sm + MISC_BASE);

  auto finish = [&](int T, f32x4 acc) {
    u32x2 xv = lds_ld<u32x2>(sm, xo + 512 * T);
    u32x2 o = {pkbf(__builtin_fmaf(acc.x, inv, bflo(xv.x)), __builtin_fmaf(acc.y, inv, bfhi(xv.x))),
               pkbf(__builtin_fmaf(acc.z, inv, bflo(xv.y)), __builtin_fmaf(acc.w, inv, bfhi(xv.y)))};
    lds_st<u32x2>(sm, xo + 512 * T, o);
    asm volatile("" ::: "memory");
    u32 n = 0;
    if (lane == 0) n = __hip_atomic_fetch_add((LAS u32*)(sm + MISC_BASE + 4 * T), 1u, __ATOMIC_RELAXED, __HIP_MEMORY_SCOPE_WORKGROUP);
    n = __builtin_amdgcn_readfirstlane(n);
    asm volatile("" ::: "memory");
    if (n == 3) {
      u32 k1, k2;
      int spin = 0;
      do { k1 = misc[12]; k2 = misc[13]; } while ((k1 == 0u || k2 == 0u) && ++spin < SPIN_CAP * 16);
      const int r1 = 2048 - (k1 & 0xfff), r2 = 2048 - (k2 & 0xfff);
      u32x2 a0 = lds_ld<u32x2>(sm, XO_BASE + 512 * T + lane * 8), a1 = lds_ld<u32x2>(sm, XO_BASE + 4096 + 512 * T + lane * 8);
      u32x2 a2 = lds_ld<u32x2>(sm, XO_BASE + 8192 + 512 * T + lane * 8), a3 = lds_ld<u32x2>(sm, XO_BASE + 12288 + 512 * T + lane * 8);
      u32x2 o0 = {__builtin_amdgcn_perm(a1.x, a0.x, 0x05040100), __builtin_amdgcn_perm(a3.x, a2.x, 0x05040100)};
      u32x2 o1 = {__builtin_amdgcn_perm(a1.x, a0.x, 0x07060302), __builtin_amdgcn_perm(a3.x, a2.x, 0x07060302)};
      u32x2 o2 = {__builtin_amdgcn_perm(a1.y, a0.y, 0x05040100), __builtin_amdgcn_perm(a3.y, a2.y, 0x05040100)};
      u32x2 o3 = {__builtin_amdgcn_perm(a1.y, a0.y, 0x07060302), __builtin_amdgcn_perm(a3.y, a2.y, 0x07060302)};
      const int row = 256 * T + 4 * lane;
      u16* op = out + row * 512 + 4 * grp;
      if (row != r1 && row != r2) *(u32x2*)op = o0;
      if (row + 1 != r1 && row + 1 != r2) *(u32x2*)(op + 512) = o1;
      if (row + 2 != r1 && row + 2 != r2) *(u32x2*)(op + 1024) = o2;
      if (row + 3 != r1 && row + 3 != r2) *(u32x2*)(op + 1536) = o3;
    }
  };
  const f32x4 z4 = {0.f, 0.f, 0.f, 0.f};
#define GROUP(j, A0, A1, A2, D0, D1, D2)                                     \
  _Pragma("unroll") for (int e = 0; e < 8; ++e) {                            \
    u32x4 f = lds_ld<u32x4>(sm, ub + 64 * (8 * (j) - 7 + e));                \
    if (D0) A0 = mma_h(K.a[7 - e], f, A0);                                   \
    if (D1) A1 = mma_h(K.a[15 - e], f, A1);                                  \
    if (D2 && e == 7) A2 = mma_h(K.a[16], f, A2);                            \
  }
  if (hb == 0) {
    f32x4 c0 = z4, c1 = z4, c2 = z4, c3 = z4, c4 = z4;
    GROUP(0, c0, c1, c2, 1, 1, 1);
    finish(0, c0);
    GROUP(1, c1, c2, c3, 1, 1, 1);
    finish(1, c1);
    GROUP(2, c2, c3, c4, 1, 1, 1);
    { int spin = 0; while (misc[8 + ch] == 0u && ++spin < SPIN_CAP * 16) {} asm volatile("" ::: "memory"); }
    c2 = far_taps<2>(sm, K, dch, ub, w2b, w3b, c2);
    finish(2, c2);
    GROUP(3, c3, c4, c4, 1, 1, 0);
    c3 = far_taps<3>(sm, K, dch, ub, w2b, w3b, c3);
    finish(3, c3);
    GROUP(4, c4, c4, c4, 1, 0, 0);
    c4 = far_taps<4>(sm, K, dch, ub, w2b, w3b, c4);
    finish(4, c4);
  } else {
    if (ch == 0) {
      u32x4 kk[4]; u32 K1, K2;
      top2(wr, tag, lane, kk, K1, K2);
      if (lane == 0) { misc[12] = K1; misc[13] = K2; }
    }
#pragma unroll
    for (int it = 0; it < 6; ++it) {
      const u32 o = dch + (it * 64 + lane) * 8 - 8;
      u32x2 ra = lds_ld<u32x2>(sm, o + U_OFF - 8), rb = lds_ld<u32x2>(sm, o + U_OFF), rc = lds_ld<u32x2>(sm, o + U_OFF + 8);
      u32 m1 = __builtin_amdgcn_alignbit(rb.x, ra.y, 16), p1 = __builtin_amdgcn_alignbit(rb.y, rb.x, 16), p3 = __builtin_amdgcn_alignbit(rc.x, rb.y, 16);
      u32x2 w3 = {hadd(hadd(m1, p1), rb.x), hadd(hadd(p1, p3), rb.y)};
      u32x2 w5 = {hadd(hadd(ra.y, rb.y), w3.x), hadd(hadd(rb.x, rc.x), w3.y)};
      lds_st<u32x2>(sm, o + W3_OFF, w3);
      lds_st<u32x2>(sm, o + W5_OFF, w5);
      if (it < 4) {
        u32 m3 = __builtin_amdgcn_alignbit(ra.y, ra.x, 16), p5 = __builtin_amdgcn_alignbit(rc.y, rc.x, 16);
        u32x2 w7 = {hadd(hadd(m3, p3), w5.x), hadd(hadd(m1, p5), w5.y)};
        u32x2 w9 = {hadd(hadd(ra.x, rc.x), w7.x), hadd(hadd(ra.y, rc.y), w7.y)};
        lds_st<u32x2>(sm, o + W7_OFF, w7);
        lds_st<u32x2>(sm, o + W9_OFF, w9);
      }
    }
    asm volatile("" ::: "memory");
    if (lane == 0) misc[8 + ch] = 1u;
    asm volatile("" ::: "memory");
    f32x4 c5 = z4, c6 = z4, c7 = z4;
    c5 = mma_h(K.a[16], lds_ld<u32x4>(sm, ub + 64 * 24), c5);
    GROUP(4, c5, c5, c6, 0, 1, 1);
    c5 = far_taps<5>(sm, K, dch, ub, w2b, w3b, c5);
    GROUP(5, c5, c6, c7, 1, 1, 1);
    finish(5, c5);
    c6 = far_taps<6>(sm, K, dch, ub, w2b, w3b, c6);
    GROUP(6, c6, c7, c7, 1, 1, 0);
    finish(6, c6);
    c7 = far_taps<7>(sm, K, dch, ub, w2b, w3b, c7);
    GROUP(7, c7, c7, c7, 1, 0, 0);
    finish(7, c7);
  }
  STAMP(6);
  float y0 = 0.f, y1 = 0.f;
  if (w == 0) {
    u32x4 pp[7];
    for (int spin = 0; spin < SPIN_CAP; ++spin) {
      bool ok = true;
#pragma unroll
      for (int j = 0; j < 7; ++j) {
        pp[j] = __builtin_amdgcn_raw_buffer_load_b128(wr, WS_PART + grp * (NPROD * 64) + lane * 16 + j * 1024, 0, 16);
        ok &= (pp[j].y == tag) & (pp[j].w == tag);
      }
      if (__all(ok)) break;
      __builtin_amdgcn_s_sleep(1);
    }
#pragma unroll
    for (int j = 0; j < 7; ++j) { y0 += __uint_as_float(pp[j].x); y1 += __uint_as_float(pp[j].z); }
    y0 += dppf<ROR(4)>(y0); y1 += dppf<ROR(4)>(y1);
    y0 += dppf<ROR(8)>(y0); y1 += dppf<ROR(8)>(y1);
    y0 = sum32(sum16(y0)); y1 = sum32(sum16(y1));
    STAMP(7);
  }
  bar();
  if (w == 0 && lane < 4) {
    const u32 key = misc[12 + (lane >> 1)];
    const int row = (2048 - (key & 0xfff)) & 2047;
    u32 rb = key & 0xffff0000u;
    rb ^= ((int)rb < 0) ? 0x80000000u : 0xffff0000u;
    const float sg = rbf(__builtin_amdgcn_rcpf(1.f + __builtin_amdgcn_exp2f(__uint_as_float(rb) * -1.44269504f)));
    const u32 oa = XO_BASE + (lane & 1) * 8192 + row * 2;
    const float o0 = bflo(lds_ld<u16>(sm, oa)), o1 = bflo(lds_ld<u16>(sm, oa + 4096));
    *(u32*)(out + row * 512 + 4 * grp + 2 * (lane & 1)) = pkbf(o0 + rbf(rbf(y0) * sg), o1 + rbf(rbf(y1) * sg));
  }
  STAMP(8);
}

extern "C" void kernel_launch(void* const* d_in, const int* in_sizes, int n_in, void* d_out, int out_size, void* d_ws,
                              size_t ws_size, hipStream_t stream) {
  fused_kernel<<<256, 512, 0, stream>>>(
      (const u16*)d_in[0], (const u16*)d_in[1], (const u16*)d_in[2], (const u16*)d_in[3], (const u16*)d_in[4],
      (const u16*)d_in[5], (const u16*)d_in[6], (const u16*)d_in[7], (const u16*)d_in[8], (const u16*)d_in[10],
      (const u16*)d_in[11], (const u16*)d_in[12], (const u16*)d_in[13], (u16*)d_out, (char*)d_ws);
}
```

```cpp
#include <hip/hip_runtime.h>
#include <stdint.h>

typedef unsigned short u16;
typedef unsigned int u32;
typedef unsigned long long u64;
typedef u32 u32x2 __attribute__((ext_vector_type(2)));
typedef u32 u32x4 __attribute__((ext_vector_type(4)));
typedef float f32x4 __attribute__((ext_vector_type(4)));
typedef float f32x2 __attribute__((ext_vector_type(2)));
typedef _Float16 h2 __attribute__((ext_vector_type(2)));
typedef _Float16 h8 __attribute__((ext_vector_type(8)));
typedef __bf16 b2 __attribute__((ext_vector_type(2)));
typedef __bf16 b8 __attribute__((ext_vector_type(8)));
typedef short s4 __attribute__((ext_vector_type(4)));
#define LAS __attribute__((address_space(3)))
#define DEV __device__ __forceinline__

#define WS_CTRL 0
#define WS_STATS 4096
#define WS_KEYS 32768
#define WS_ACKS 65536
#define WS_PART 131072
#define WS_BYTES (2u << 20)
#define NPROD 112
#define SPIN_CAP 100000

#define TAB_RS 2080
#define KL_BASE 112320
#define KL_CS 1984
#define KL_CH (4 * KL_CS)
#define XO_BASE (KL_BASE + 4 * KL_CH)
#define MISC_BASE (XO_BASE + 16384)
#define LDS_BYTES (MISC_BASE + 256)
#define DCH 14944
#define U_OFF 480
#define W5_OFF (4576 + 32)
#define W3_OFF (W5_OFF + 3104)
#define W9_OFF (W3_OFF + 3104)
#define W7_OFF (W9_OFF + 2080)
#define KG0 592
#define KGS 52
#define KG4 (KG0 + 4 * KGS)
#define KG5 (KG4 + 84)

#define STAMP(i)
#define STAMP_DECL
#define STAMP_END(i)

DEV float bflo(u32 v) { return __uint_as_float(v << 16); }
DEV float bfhi(u32 v) { return __uint_as_float(v & 0xffff0000u); }
DEV u32 pkbf(float a, float b) { f32x2 f = {a, b}; return __builtin_bit_cast(u32, __builtin_convertvector(f, b2)); }
DEV float rbf(float a) { return bflo(pkbf(a, a)); }
DEV u32 pkh(float a, float b) { h2 h = {(_Float16)a, (_Float16)b}; return __builtin_bit_cast(u32, h); }
DEV u32 hadd(u32 a, u32 b) { return __builtin_bit_cast(u32, __builtin_bit_cast(h2, a) + __builtin_bit_cast(h2, b)); }
DEV f32x4 mma_h(u32x4 a, u32x4 b, f32x4 c) {
  return __builtin_amdgcn_mfma_f32_16x16x32_f16(__builtin_bit_cast(h8, a), __builtin_bit_cast(h8, b), c, 0, 0, 0);
}
DEV f32x4 mma_b(u32x4 a, u32x4 b, f32x4 c) {
  return __builtin_amdgcn_mfma_f32_16x16x32_bf16(__builtin_bit_cast(b8, a), __builtin_bit_cast(b8, b), c, 0, 0, 0);
}
template <int C> DEV u32 dppu(u32 v) { return __builtin_amdgcn_update_dpp(0, v, C, 0xf, 0xf, true); }
template <int C> DEV float dppf(float v) { return __uint_as_float(dppu<C>(__float_as_uint(v))); }
#define ROR(n) (0x120 + (n))
DEV float sum16(float v) { u32 u = __float_as_uint(v); auto r = __builtin_amdgcn_permlane16_swap(u, u, false, false); return __uint_as_float(r[0]) + __uint_as_float(r[1]); }
DEV float sum32(float v) { u32 u = __float_as_uint(v); auto r = __builtin_amdgcn_permlane32_swap(u, u, false, false); return __uint_as_float(r[0]) + __uint_as_float(r[1]); }
DEV u32 rowmax(u32 v) {
  v = max(v, dppu<ROR(1)>(v)); v = max(v, dppu<ROR(2)>(v)); v = max(v, dppu<ROR(4)>(v)); v = max(v, dppu<ROR(8)>(v));
  return v;
}
DEV u32 wavemax(u32 v) {
  v = rowmax(v);
  u32 a = __builtin_amdgcn_readlane(v, 0), b = __builtin_amdgcn_readlane(v, 16), c = __builtin_amdgcn_readlane(v, 32), d = __builtin_amdgcn_readlane(v, 48);
  return max(max(a, b), max(c, d));
}
DEV void bar() { asm volatile("s_waitcnt lgkmcnt(0)\n\ts_barrier" ::: "memory"); }

struct cf { float r, i; };
DEV cf cmul(cf a, cf b) { cf o; o.r = __builtin_fmaf(a.r, b.r, -(a.i * b.i)); o.i = __builtin_fmaf(a.r, b.i, a.i * b.r); return o; }
DEV cf csq(cf a) { cf o; o.r = __builtin_fmaf(a.r, a.r, -(a.i * a.i)); o.i = (a.r + a.r) * a.i; return o; }
DEV cf seed(float p2, float th, float tl, float v) {
  float e = __builtin_amdgcn_exp2f(p2 * v);
  float f = __builtin_fmaf(tl, v, __builtin_amdgcn_fractf(th * v));
  cf o; o.r = e * __builtin_amdgcn_cosf(f); o.i = e * __builtin_amdgcn_sinf(f); return o;
}

template <typename T> DEV T lds_ld(LAS char* s, u32 off) { return *(LAS T*)(s + off); }
template <typename T> DEV void lds_st(LAS char* s, u32 off, T v) { *(LAS T*)(s + off) = v; }
DEV u32x2 lds_tr(LAS char* s, u32 off) { return __builtin_bit_cast(u32x2, __builtin_amdgcn_ds_read_tr16_b64_v4i16((LAS s4*)(s + off))); }
DEV u32x4 lds_tr2(LAS char* s, u32 o0, u32 o1) { u32x2 a = lds_tr(s, o0), b = lds_tr(s, o1); u32x4 r = {a.x, a.y, b.x, b.y}; return r; }

DEV void top2(__amdgpu_buffer_rsrc_t wr, u32 tag, int lane, u32x4 (&kk)[4], u32& K1, u32& K2) {
  for (int spin = 0; spin < SPIN_CAP; ++spin) {
    bool ok = true;
#pragma unroll
    for (int j = 0; j < 4; ++j) {
      kk[j] = __builtin_amdgcn_raw_buffer_load_b128(wr, WS_KEYS + (lane >> 1) * 64 + (lane & 1) * 16 + j * 2048, 0, 16);
      ok &= (kk[j].y == tag) & (kk[j].w == tag);
    }
    if (__all(ok)) break;
    __builtin_amdgcn_s_sleep(1);
  }
  K1 = wavemax(max(max(kk[0].x, kk[1].x), max(kk[2].x, kk[3].x)));
  u32 m = 0;
#pragma unroll
  for (int j = 0; j < 4; ++j) m = max(m, kk[j].x == K1 ? 0u : kk[j].x);
  K2 = wavemax(m);
}

struct KFr { u32x4 a[18], s5[3], s3[3], t[4]; };

template <int T> DEV f32x4 far_taps(LAS char* sm, const KFr& K, u32 dch, u32 ub, u32 w2b, u32 w3b, f32x4 acc) {
#pragma unroll
  for (int i = 0; i < 3; ++i) {
    const int u = T + i - 4;
    if (u >= 0) {
      acc = mma_h(lds_tr2(sm, w2b + W5_OFF + 512 * u, w2b + W5_OFF + 512 * u + 128), K.s5[i], acc);
      acc = mma_h(lds_tr2(sm, w2b + W3_OFF + 8 + 512 * u, w2b + W3_OFF + 8 + 512 * u + 128), K.s3[i], acc);
    }
  }
  if (T >= 4) {
#pragma unroll
    for (int i = 0; i <= T - 4; ++i) {
      const int u = T - 4 - i;
      acc = mma_h(lds_tr2(sm, w3b + 512 * u, w3b + 512 * u + 256), K.t[i], acc);
    }
    acc = mma_h(K.a[17], lds_ld<u32x4>(sm, ub + 2 * (256 * T - 1032 + 16)), acc);
  }
  return acc;
}

__global__ __launch_bounds__(512, 2) void fused_kernel(
    const u16* __restrict__ x, const u16* __restrict__ pAre, const u16* __restrict__ pAim, const u16* __restrict__ pBre,
    const u16* __restrict__ pBim, const u16* __restrict__ pCre, const u16* __restrict__ pCim, const u16* __restrict__ pLdt,
    const u16* __restrict__ pDs, const u16* __restrict__ pWr, const u16* __restrict__ pWg, const u16* __restrict__ pWu,
    const u16* __restrict__ pWd, u16* __restrict__ out, char* ws) {
  __shared__ __attribute__((aligned(16))) char smem[LDS_BYTES];
  LAS char* sm = (LAS char*)smem;
  const int tid = threadIdx.x, lane = tid & 63;
  const int w = __builtin_amdgcn_readfirstlane(tid >> 6);
  const int c = lane & 15, g = lane >> 4, q = (lane >> 2) & 3, p = lane & 3;
  const int b = blockIdx.x;
  const __amdgpu_buffer_rsrc_t wr = __builtin_amdgcn_make_buffer_rsrc(ws, 0, WS_BYTES, 0x00020000);
  STAMP_DECL;
  STAMP(0);

  if (b < 128) {
    const u16* xr = x + (16 * b + c) * 512 + 64 * w + 16 * g;
    u32x4 x0 = *(const u32x4*)xr, x1 = *(const u32x4*)(xr + 8);
    u32x4 w0 = *(const u32x4*)(pWr + 64 * w + 16 * g), w1 = *(const u32x4*)(pWr + 64 * w + 16 * g + 8);
    const u32 tag = __builtin_amdgcn_raw_buffer_load_b32(wr, WS_CTRL, 0, 16) + 1u;
    u32x4 wa[4], wb[2];
    const int bb = b < NPROD ? b : 0;
    {
      const u16* src = (w & 4 ? pWu : pWg) + (128 * (w & 3) + (lane >> 1)) * 1792 + 16 * bb + 8 * (lane & 1);
#pragma unroll
      for (int j = 0; j < 4; ++j) wa[j] = *(const u32x4*)(src + j * 32 * 1792);
      const u16* sd = pWd + (16 * bb + (lane >> 3)) * 512 + 64 * w + 8 * (lane & 7);
#pragma unroll
      for (int j = 0; j < 2; ++j) wb[j] = *(const u32x4*)(sd + j * 8 * 512);
    }
    const u32x4 ones = {0x3f803f80u, 0x3f803f80u, 0x3f803f80u, 0x3f803f80u};
    const bool selw = (c & 1) != 0;
    u32x4 a0 = selw ? w0 : ones, a1 = selw ? w1 : ones;
    const f32x4 z4 = {0.f, 0.f, 0.f, 0.f};
    f32x4 d3 = mma_b(a1, ones, mma_b(a0, ones, z4));
    f32x4 d1 = mma_b(a1, x1, mma_b(a0, x0, z4));
    f32x4 d2 = mma_b(x1, x1, mma_b(x0, x0, z4));
    {
      float dg = p == 0 ? d2.x : p == 1 ? d2.y : p == 2 ? d2.z : d2.w;
      f32x4 pr = {d1.x, d1.y, dg, d3.y};
      if (g == q) lds_st<f32x4>(sm, w * 256 + c * 16, pr);
    }
    STAMP(1);
    bar();
    if (w == 0) {
      f32x4 s = lds_ld<f32x4>(sm, g * 512 + c * 16) + lds_ld<f32x4>(sm, g * 512 + 256 + c * 16);
      s.x = sum32(sum16(s.x)); s.y = sum32(sum16(s.y)); s.z = sum32(sum16(s.z)); s.w = sum32(sum16(s.w));
      float mean = s.x * (1.f / 512.f);
      float var = fmaxf(__builtin_fmaf(-mean, mean, s.z * (1.f / 512.f)), 0.f);
      float sd = __builtin_amdgcn_sqrtf(rbf(rbf(var) + 1e-5f));
      u32 ms = pkbf(mean, sd);
      u32 msn = dppu<0xb1>(ms);
      u32x4 gs = {ms, tag, msn, tag};
      if (lane < 16 && !(lane & 1)) __builtin_amdgcn_raw_buffer_store_b128(gs, wr, WS_STATS + b * 128 + lane * 8, 0, 16);
      float r = __builtin_fmaf(-bflo(ms), s.w, s.y) * __builtin_amdgcn_rcpf(bfhi(ms));
      u32 rb = pkbf(r, r) & 0xffff0000u;
      rb ^= ((int)rb < 0) ? 0xffff0000u : 0x80000000u;
      u32 key = rb | (u32)(2048 - (16 * b + c));
      u32 k1 = rowmax(key);
      u32 k2 = rowmax(key == k1 ? 0u : key);
      u32x4 gk = {key, tag, ms, tag};
      if (lane < 16 && key >= k2) __builtin_amdgcn_raw_buffer_store_b128(gk, wr, WS_KEYS + b * 64 + (key == k1 ? 0 : 16), 0, 16);
      STAMP(2);
    }
    if (b >= NPROD) {
      if (b == 127 && w == 0) {
        u32x4 kk[4]; u32 K1, K2;
        top2(wr, tag, lane, kk, K1, K2);
        for (int spin = 0; spin < SPIN_CAP; ++spin) {
          u32x2 a = __builtin_amdgcn_raw_buffer_load_b64(wr, WS_ACKS + lane * 8, 0, 16);
          if (__all(a.x == tag && a.y == tag)) break;
          __builtin_amdgcn_s_sleep(2);
        }
        if (lane == 0) __builtin_amdgcn_raw_buffer_store_b32(tag, wr, WS_CTRL, 0, 16);
      }
      STAMP_END(15);
      return;
    }
    const u32 wbase = 8192 + w * 6144;
#pragma unroll
    for (int j = 0; j < 4; ++j) lds_st<u32x4>(sm, wbase + j * 1024 + lane * 16, wa[j]);
#pragma unroll
    for (int j = 0; j < 2; ++j) lds_st<u32x4>(sm, wbase + 4096 + j * 1024 + lane * 16, wb[j]);
    u32x4 fa[4]; u32x2 fb[4];
#pragma unroll
    for (int j = 0; j < 4; ++j) {
      fa[j] = lds_tr2(sm, wbase + j * 1024 + (8 * g + q) * 32 + 8 * p, wbase + j * 1024 + (8 * g + q) * 32 + 8 * p + 128);
      fb[j] = lds_tr(sm, wbase + 4096 + (4 * g + q) * 128 + j * 32 + 8 * p);
    }
    STAMP(3);
    if (w == 0) {
      u32x4 kk[4]; u32 K1, K2;
      top2(wr, tag, lane, kk, K1, K2);
#pragma unroll
      for (int j = 0; j < 4; ++j)
        if (kk[j].x >= K2) { u32x2 v = {kk[j].x, kk[j].z}; lds_st<u32x2>(sm, 4096 + (kk[j].x == K1 ? 0 : 8), v); }
    }
    bar();
    STAMP(4);
    {
      u32x2 sel = lds_ld<u32x2>(sm, 4096 + (w >> 2) * 8);
      int row = (2048 - (sel.x & 0xfff)) & 2047;
      u32 xv = *(const u32*)(x + row * 512 + 2 * (tid & 255));
      float m = bflo(sel.y), rs = __builtin_amdgcn_rcpf(bfhi(sel.y));
      u32 t = pkbf(bflo(xv) - m, bfhi(xv) - m);
      lds_st<u32>(sm, 4352 + tid * 4 + (w >> 2) * 16, pkbf(bflo(t) * rs, bfhi(t) * rs));
    }
    bar();
    STAMP(5);
    {
      const u32 xb = 4352 + ((c >> 2) & 1) * 1040 + (w & 3) * 256 + g * 16;
      f32x4 d = z4;
#pragma unroll
      for (int j = 0; j < 4; ++j) d = mma_b(fa[j], lds_ld<u32x4>(sm, xb + j * 64), d);
      if ((c & 3) == (w & 3) && (c >> 3) == (w >> 2)) lds_st<f32x4>(sm, c * 64 + g * 16, d);
    }
    bar();
    {
      f32x4 s = lds_ld<f32x4>(sm, c * 64 + g * 16);
      float hv[4];
#pragma unroll
      for (int i = 0; i < 4; ++i) {
        float v = s[i];
        v += dppf<0xb1>(v);
        v += dppf<0x4e>(v);
        v = rbf(v);
        float o = dppf<ROR(8)>(v);
        float ga = (c & 8) ? o : v, up = (c & 8) ? v : o;
        float sl = ga * __builtin_amdgcn_rcpf(1.f + __builtin_amdgcn_exp2f(ga * -1.44269504f));
        hv[i] = rbf(sl) * up;
      }
      u32x2 h = {pkbf(hv[0], hv[1]), pkbf(hv[2], hv[3])};
      const u32x2 z2 = {0u, 0u};
      f32x4 d = z4;
#pragma unroll
      for (int j = 0; j < 4; ++j)
        d = __builtin_amdgcn_mfma_f32_16x16x16bf16_1k(__builtin_bit_cast(s4, fb[j]), __builtin_bit_cast(s4, (c & 3) == j ? h : z2), d, 0, 0, 0);
      u32x4 gp = {__float_as_uint((c & 8) ? d.z : d.x), tag, __float_as_uint((c & 8) ? d.w : d.y), tag};
      const u32 grp = 16 * w + 4 * (c & 3) + g;
      __builtin_amdgcn_raw_buffer_store_b128(gp, wr, WS_PART + (grp * NPROD + b) * 64 + ((c >> 2) & 1) * 32 + (c >> 3) * 16, 0, 16);
    }
    STAMP(6);
    STAMP_END(15);
    return;
  }

  const int cb = b - 128;
  const int grp = (cb & 7) * 16 + (cb >> 3);
  const int ch = w & 3, hb = w >> 2;
  const int chan = 4 * grp + ch;
  const u32 vAre = *(const u32*)(pAre + chan * 128 + 2 * lane), vAim = *(const u32*)(pAim + chan * 128 + 2 * lane);
  const u32 vBre = *(const u32*)(pBre + chan * 128 + 2 * lane), vBim = *(const u32*)(pBim + chan * 128 + 2 * lane);
  const u32 vCre = *(const u32*)(pCre + chan * 128 + 2 * lane), vCim = *(const u32*)(pCim + chan * 128 + 2 * lane);
  const u32 vld = pLdt[chan], vds = pDs[chan];
  const u32 tag = __builtin_amdgcn_raw_buffer_load_b32(wr, WS_CTRL, 0, 16) + 1u;
  u32x2 xc[4];
#pragma unroll
  for (int i = 0; i < 4; ++i) xc[i] = *(const u32x2*)(x + (4 * tid + i) * 512 + 4 * grp);
  {
    const u32x4 z = {0u, 0u, 0u, 0u};
    if (tid < 480) lds_st<u32x4>(sm, KL_BASE + (tid / 120) * KL_CH + (tid % 120) * 16, z);
    if (tid >= 496) { const u32x4 mi = {0u, 0u, 0u, 0u}; lds_st<u32x4>(sm, MISC_BASE + (tid - 496) * 16, mi); }
  }
  float inv, dsc;
  {
    const float dt = rbf(__builtin_amdgcn_exp2f(bflo(vld) * 1.44269504f));
    const float d1 = 1.f + bflo(vds);
    const float dl = dt * 1.44269504f;
    float b0 = (fabsf(bflo(vCre)) + fabsf(bflo(vCim))) * (fabsf(bflo(vBre)) + fabsf(bflo(vBim))) * __builtin_amdgcn_exp2f(fmaxf(bflo(vAre), 0.f) * dl * 2048.f);
    float b1 = (fabsf(bfhi(vCre)) + fabsf(bfhi(vCim))) * (fabsf(bfhi(vBre)) + fabsf(bfhi(vBim))) * __builtin_amdgcn_exp2f(fmaxf(bfhi(vAre), 0.f) * dl * 2048.f);
    float bd = fmaxf(fmaxf(b0, b1) * dt, fabsf(d1) * (1.f / 128.f));
    int be = (int)(wavemax(__float_as_uint(bd)) >> 23);
    int se = min(max(260 - be, 1), 253);
    const float sc = __uint_as_float((u32)se << 23);
    inv = __uint_as_float((u32)(254 - se) << 23);
    dsc = d1 * sc;
    const float are = hb ? bfhi(vAre) : bflo(vAre), aim = hb ? bfhi(vAim) : bflo(vAim);
    cf B = {(hb ? bfhi(vBre) : bflo(vBre)) * dt, (hb ? bfhi(vBim) : bflo(vBim)) * dt};
    cf C = {(hb ? bfhi(vCre) : bflo(vCre)) * sc, (hb ? bfhi(vCim) : bflo(vCim)) * sc};
    const float p2 = (dt * are) * 1.44269504f;
    const float qq = dt * aim;
    const float th = qq * 0.15915494f;
    const float tl = __builtin_fmaf(qq, 6.4206382e-9f, __builtin_fmaf(qq, 0.15915494f, -th));
    const u32 tb = ch * 512 + hb * 256 + lane * 4;
#define WQ(bi, v) lds_st<u32>(sm, tb + (((bi) & 1) * 16 + ((bi) >> 1)) * TAB_RS, pkh((v).r, -(v).i))
#define WP(ri, v) lds_st<u32>(sm, tb + (32 + (ri)) * TAB_RS, pkh((v).r, (v).i))
    cf E1 = seed(p2, th, tl, 1.f), E16 = seed(p2, th, tl, 16.f), E256 = seed(p2, th, tl, 256.f);
    cf E2 = csq(E1), E4 = csq(E2), E8 = csq(E4), E32 = csq(E16), E64 = csq(E32), E128 = csq(E64), E512 = csq(E256), E1k = csq(E512);
    cf Q[16];
    Q[0] = cf{1.f, 0.f}; Q[1] = E8; Q[2] = E16; Q[3] = cmul(E8, E16); Q[4] = E32;
#pragma unroll
    for (int i = 5; i < 8; ++i) Q[i] = cmul(Q[i - 4], E32);
    Q[8] = E64;
#pragma unroll
    for (int i = 9; i < 16; ++i) Q[i] = cmul(Q[i - 8], E64);
#pragma unroll
    for (int i = 0; i < 16; ++i) WQ(i, Q[i]);
    WQ(16, E128);
#pragma unroll
    for (int i = 17; i < 32; ++i) { cf t = cmul(Q[i - 16], E128); WQ(i, t); }
    cf P0 = cmul(C, B), P1 = cmul(P0, E1), P2 = cmul(P0, E2), P3 = cmul(P1, E2), P4 = cmul(P0, E4);
    cf P5 = cmul(P1, E4), P6 = cmul(P2, E4), P7 = cmul(P3, E4);
    WP(0, P0); WP(1, P1); WP(2, P2); WP(3, P3); WP(4, P4); WP(5, P5); WP(6, P6); WP(7, P7);
    cf R0 = cmul(P0, E256), R2 = cmul(P2, E256), R4 = cmul(P4, E256), R6 = cmul(P6, E256), R8 = cmul(R0, E8);
    WP(8, R0); WP(9, R2); WP(10, R4); WP(11, R6); WP(12, R8);
    cf S0 = cmul(P0, E512), S1 = cmul(R0, E512), S2 = cmul(P4, E512), S3 = cmul(R4, E512);
    WP(13, S0); WP(14, S1); WP(15, S2); WP(16, S3);
    cf T0 = cmul(P0, E1k), T1 = cmul(R0, E1k), T2 = cmul(S0, E1k), T3 = cmul(S1, E1k), T4 = cmul(T0, E1k);
    WP(17, T0); WP(18, T1); WP(19, T2); WP(20, T3); WP(21, T4);
  }
  if (tid == 0) __builtin_amdgcn_raw_buffer_store_b32(tag, wr, WS_ACKS + cb * 4, 0, 16);
  STAMP(1);
  bar();
  {
    const int ra = c < 8 ? c : (int)((0xcbaaa988u >> ((c - 8) * 4)) & 15);
    const int rb = c < 9 ? 13 + c : (c == 12 ? 13 : 17);
    const u32 qa = ch * 512 + g * 16 + (hb * 16 + c) * TAB_RS;
    const u32 pa = ch * 512 + g * 16 + (32 + ra) * TAB_RS;
    const u32 pb = ch * 512 + g * 16 + (32 + rb) * TAB_RS;
    f32x4 da = {0.f, 0.f, 0.f, 0.f}, db = da;
#pragma unroll
    for (int k = 0; k < 8; ++k) {
      u32x4 fq = lds_ld<u32x4>(sm, qa + k * 64);
      da = mma_h(lds_ld<u32x4>(sm, pa + k * 64), fq, da);
      db = mma_h(fq, lds_ld<u32x4>(sm, pb + k * 64), db);
    }
    if (lane == 0 && hb == 0) da.x += dsc;
    const u32 kl = KL_BASE + ch * KL_CH;
    u32x2 va = {pkh(da.x, da.y), pkh(da.z, da.w)};
    lds_st<u32x2>(sm, kl + 2 * (16 + (g >> 1) * 256 + 16 * c + 8 * hb + 4 * (g & 1)), va);
    u32x2 vb = {pkh(db.x, db.y), pkh(db.z, db.w)};
    const u32 h0 = vb.x & 0xffffu;
    int dst = c < 2 ? KG0 + hb * KGS + 16 * c : c < 4 ? KG0 + (3 - hb) * KGS + 16 * (c - 2) : (hb ? KG5 : KG4) + 16 * (c - 4);
    dst += 4 * g;
    if (hb == 0 && g == 0) {
      if (c == 0 || c == 4) vb.x &= 0xffff0000u;
      if (c >= 8) { vb.x = h0; vb.y = 0u; }
      if (c == 9) dst = KG0 + 32;
      if (c == 10) dst = 572;
      if (c == 11) { dst = 568; vb.x = 0u; vb.y = h0 << 16; }
      if (c == 12) { dst = 528; vb.x = h0 | (h0 << 16); vb.y = h0; }
    }
    if (c < 8 || (hb == 0 && g == 0 && c <= 12)) lds_st<u32x2>(sm, kl + 2 * dst, vb);
  }
  bar();
  {
    const u32 kl = KL_BASE + ch * KL_CH;
#pragma unroll
    for (int it = 0; it < 2; ++it) {
      const int xk = it * 128 + hb * 64 + lane;
      if (xk < 238) {
        u32x2 r0 = lds_ld<u32x2>(sm, kl + xk * 8), r1 = lds_ld<u32x2>(sm, kl + xk * 8 + 8);
        u32 m0 = __builtin_amdgcn_alignbit(r0.y, r0.x, 16), m1 = __builtin_amdgcn_alignbit(r1.x, r0.y, 16), m2 = __builtin_amdgcn_alignbit(r1.y, r1.x, 16);
        u32x2 c1 = {m0, m1}, c2 = {r0.y, r1.x}, c3 = {m1, m2};
        lds_st<u32x2>(sm, kl + KL_CS + xk * 8, c1);
        lds_st<u32x2>(sm, kl + 2 * KL_CS + xk * 8, c2);
        lds_st<u32x2>(sm, kl + 3 * KL_CS + xk * 8, c3);
      }
    }
    const u32x2 z2 = {0u, 0u};
    if (hb == 0) { if (lane < 60) lds_st<u32x2>(sm, ch * DCH + lane * 8, z2); }
    else if (lane < 16) {
      const u32 o = q == 0 ? W5_OFF : q == 1 ? W3_OFF : q == 2 ? W9_OFF : W7_OFF;
      lds_st<u32x2>(sm, ch * DCH + o - 32 + p * 8, z2);
    }
  }
  bar();
  STAMP(2);
  KFr K;
  {
    const u32 kl = KL_BASE + ch * KL_CH + 8 * p;
    {
      const int io = 32 - 8 * g - q - 4, sg = io & 3;
      const u32 a = kl + sg * KL_CS + 2 * (io - sg);
#pragma unroll
      for (int t = 0; t < 18; ++t) K.a[t] = lds_tr2(sm, a + 64 * t + 8, a + 64 * t);
    }
    {
      const int kap = 16 * (g >> 1) + 4 * (g & 1) + q + 8;
      const int n0 = 130 - 64 - kap, par = n0 & 1, a0 = n0 >> 1;
      const int i5 = KG0 + par * KGS + a0 - 32, i3 = par ? KG0 + 3 * KGS + a0 - 32 : KG0 + 2 * KGS + a0 - 33;
      const u32 a5 = kl + (i5 & 3) * KL_CS + 2 * (i5 & ~3), a3 = kl + (i3 & 3) * KL_CS + 2 * (i3 & ~3);
#pragma unroll
      for (int i = 0; i < 3; ++i) {
        K.s5[i] = lds_tr2(sm, a5 + 32 * (2 - i) + 8, a5 + 32 * (2 - i));
        K.s3[i] = lds_tr2(sm, a3 + 32 * (2 - i) + 8, a3 + 32 * (2 - i));
      }
    }
    {
      const int n0 = 65 - (8 + 4 * (g & 1) + q);
      const int it = g < 2 ? KG4 + n0 - 64 : KG5 + n0 - 65;
      const u32 a = kl + (it & 3) * KL_CS + 2 * (it & ~3);
#pragma unroll
      for (int i = 0; i < 4; ++i) K.t[i] = lds_tr2(sm, a + 32 * i + 16, a + 32 * i);
    }
  }
  u32x2 xt[4];
  xt[0] = u32x2{__builtin_amdgcn_perm(xc[1].x, xc[0].x, 0x05040100), __builtin_amdgcn_perm(xc[3].x, xc[2].x, 0x05040100)};
  xt[1] = u32x2{__builtin_amdgcn_perm(xc[1].x, xc[0].x, 0x07060302), __builtin_amdgcn_perm(xc[3].x, xc[2].x, 0x07060302)};
  xt[2] = u32x2{__builtin_amdgcn_perm(xc[1].y, xc[0].y, 0x05040100), __builtin_amdgcn_perm(xc[3].y, xc[2].y, 0x05040100)};
  xt[3] = u32x2{__builtin_amdgcn_perm(xc[1].y, xc[0].y, 0x07060302), __builtin_amdgcn_perm(xc[3].y, xc[2].y, 0x07060302)};
#pragma unroll
  for (int i = 0; i < 4; ++i) lds_st<u32x2>(sm, XO_BASE + i * 4096 + tid * 8, xt[i]);
  STAMP(3);
  {
    u32x4 s0, s1;
    for (int spin = 0; spin < SPIN_CAP; ++spin) {
      s0 = __builtin_amdgcn_raw_buffer_load_b128(wr, WS_STATS + tid * 32, 0, 16);
      s1 = __builtin_amdgcn_raw_buffer_load_b128(wr, WS_STATS + tid * 32 + 16, 0, 16);
      if (__all((s0.y == tag) & (s0.w == tag) & (s1.y == tag) & (s1.w == tag))) break;
      __builtin_amdgcn_s_sleep(1);
    }
    STAMP(4);
    const float r0 = __builtin_amdgcn_rcpf(bfhi(s0.x)), r1 = __builtin_amdgcn_rcpf(bfhi(s0.z));
    const float r2 = __builtin_amdgcn_rcpf(bfhi(s1.x)), r3 = __builtin_amdgcn_rcpf(bfhi(s1.z));
    const float n0 = -bflo(s0.x) * r0, n1 = -bflo(s0.z) * r1, n2 = -bflo(s1.x) * r2, n3 = -bflo(s1.z) * r3;
#pragma unroll
    for (int i = 0; i < 4; ++i) {
      u32x2 u = {pkh(__builtin_fmaf(bflo(xt[i].x), r0, n0), __builtin_fmaf(bfhi(xt[i].x), r1, n1)),
                 pkh(__builtin_fmaf(bflo(xt[i].y), r2, n2), __builtin_fmaf(bfhi(xt[i].y), r3, n3))};
      lds_st<u32x2>(sm, i * DCH + U_OFF + tid * 8, u);
    }
  }
  bar();
  STAMP(5);
  const u32 dch = ch * DCH;
  const u32 ub = dch + U_OFF - 32 + 32 * c + 16 * g;
  const u32 w2b = dch - 32 + (16 * (g >> 1) + 4 * (g & 1) + q) * 16 + 8 * p;
  const u32 w3b = dch + (g < 2 ? W9_OFF : W7_OFF + 16) - 32 + (4 * (g & 1) + q) * 32 + 8 * p;
  const u32 xo = XO_BASE + ch * 4096 + 32 * c + 8 * g;
  volatile LAS u32* misc = (volatile LAS u32*)(sm + MISC_BASE);

  auto finish = [&](int T, f32x4 acc) {
    u32x2 xv = lds_ld<u32x2>(sm, xo + 512 * T);
    u32x2 o = {pkbf(__builtin_fmaf(acc.x, inv, bflo(xv.x)), __builtin_fmaf(acc.y, inv, bfhi(xv.x))),
               pkbf(__builtin_fmaf(acc.z, inv, bflo(xv.y)), __builtin_fmaf(acc.w, inv, bfhi(xv.y)))};
    lds_st<u32x2>(sm, xo + 512 * T, o);
    asm volatile("" ::: "memory");
    u32 n = 0;
    if (lane == 0) n = __hip_atomic_fetch_add((LAS u32*)(sm + MISC_BASE + 4 * T), 1u, __ATOMIC_RELAXED, __HIP_MEMORY_SCOPE_WORKGROUP);
    n = __builtin_amdgcn_readfirstlane(n);
    asm volatile("" ::: "memory");
    if (n == 3) {
      u32 k1, k2;
      int spin = 0;
      do { k1 = misc[12]; k2 = misc[13]; } while ((k1 == 0u || k2 == 0u) && ++spin < SPIN_CAP * 16);
      const int r1 = 2048 - (k1 & 0xfff), r2 = 2048 - (k2 & 0xfff);
      u32x2 a0 = lds_ld<u32x2>(sm, XO_BASE + 512 * T + lane * 8), a1 = lds_ld<u32x2>(sm, XO_BASE + 4096 + 512 * T + lane * 8);
      u32x2 a2 = lds_ld<u32x2>(sm, XO_BASE + 8192 + 512 * T + lane * 8), a3 = lds_ld<u32x2>(sm, XO_BASE + 12288 + 512 * T + lane * 8);
      u32x2 o0 = {__builtin_amdgcn_perm(a1.x, a0.x, 0x05040100), __builtin_amdgcn_perm(a3.x, a2.x, 0x05040100)};
      u32x2 o1 = {__builtin_amdgcn_perm(a1.x, a0.x, 0x07060302), __builtin_amdgcn_perm(a3.x, a2.x, 0x07060302)};
      u32x2 o2 = {__builtin_amdgcn_perm(a1.y, a0.y, 0x05040100), __builtin_amdgcn_perm(a3.y, a2.y, 0x05040100)};
      u32x2 o3 = {__builtin_amdgcn_perm(a1.y, a0.y, 0x07060302), __builtin_amdgcn_perm(a3.y, a2.y, 0x07060302)};
      const int row = 256 * T + 4 * lane;
      u16* op = out + row * 512 + 4 * grp;
      if (row != r1 && row != r2) *(u32x2*)op = o0;
      if (row + 1 != r1 && row + 1 != r2) *(u32x2*)(op + 512) = o1;
      if (row + 2 != r1 && row + 2 != r2) *(u32x2*)(op + 1024) = o2;
      if (row + 3 != r1 && row + 3 != r2) *(u32x2*)(op + 1536) = o3;
    }
  };
  const f32x4 z4 = {0.f, 0.f, 0.f, 0.f};
#define GROUP(j, A0, A1, A2, D0, D1, D2)                                     \
  _Pragma("unroll") for (int e = 0; e < 8; ++e) {                            \
    u32x4 f = lds_ld<u32x4>(sm, ub + 64 * (8 * (j) - 7 + e));                \
    if (D0) A0 = mma_h(K.a[7 - e], f, A0);                                   \
    if (D1) A1 = mma_h(K.a[15 - e], f, A1);                                  \
    if (D2 && e == 7) A2 = mma_h(K.a[16], f, A2);                            \
  }
  if (hb == 0) {
    f32x4 c0 = z4, c1 = z4, c2 = z4, c3 = z4, c4 = z4;
    GROUP(0, c0, c1, c2, 1, 1, 1);
    finish(0, c0);
    GROUP(1, c1, c2, c3, 1, 1, 1);
    finish(1, c1);
    GROUP(2, c2, c3, c4, 1, 1, 1);
    { int spin = 0; while (misc[8 + ch] == 0u && ++spin < SPIN_CAP * 16) {} asm volatile("" ::: "memory"); }
    c2 = far_taps<2>(sm, K, dch, ub, w2b, w3b, c2);
    finish(2, c2);
    GROUP(3, c3, c4, c4, 1, 1, 0);
    c3 = far_taps<3>(sm, K, dch, ub, w2b, w3b, c3);
    finish(3, c3);
    GROUP(4, c4, c4, c4, 1, 0, 0);
    c4 = far_taps<4>(sm, K, dch, ub, w2b, w3b, c4);
    finish(4, c4);
  } else {
    if (ch == 0) {
      u32x4 kk[4]; u32 K1, K2;
      top2(wr, tag, lane, kk, K1, K2);
      if (lane == 0) { misc[12] = K1; misc[13] = K2; }
    }
#pragma unroll
    for (int it = 0; it < 6; ++it) {
      const u32 o = dch + (it * 64 + lane) * 8 - 8;
      u32x2 ra = lds_ld<u32x2>(sm, o + U_OFF - 8), rb = lds_ld<u32x2>(sm, o + U_OFF), rc = lds_ld<u32x2>(sm, o + U_OFF + 8);
      u32 m1 = __builtin_amdgcn_alignbit(rb.x, ra.y, 16), p1 = __builtin_amdgcn_alignbit(rb.y, rb.x, 16), p3 = __builtin_amdgcn_alignbit(rc.x, rb.y, 16);
      u32x2 w3 = {hadd(hadd(m1, p1), rb.x), hadd(hadd(p1, p3), rb.y)};
      u32x2 w5 = {hadd(hadd(ra.y, rb.y), w3.x), hadd(hadd(rb.x, rc.x), w3.y)};
      lds_st<u32x2>(sm, o + W3_OFF, w3);
      lds_st<u32x2>(sm, o + W5_OFF, w5);
      if (it < 4) {
        u32 m3 = __builtin_amdgcn_alignbit(ra.y, ra.x, 16), p5 = __builtin_amdgcn_alignbit(rc.y, rc.x, 16);
        u32x2 w7 = {hadd(hadd(m3, p3), w5.x), hadd(hadd(m1, p5), w5.y)};
        u32x2 w9 = {hadd(hadd(ra.x, rc.x), w7.x), hadd(hadd(ra.y, rc.y), w7.y)};
        lds_st<u32x2>(sm, o + W7_OFF, w7);
        lds_st<u32x2>(sm, o + W9_OFF, w9);
      }
    }
    asm volatile("" ::: "memory");
    if (lane == 0) misc[8 + ch] = 1u;
    asm volatile("" ::: "memory");
    f32x4 c5 = z4, c6 = z4, c7 = z4;
    c5 = mma_h(K.a[16], lds_ld<u32x4>(sm, ub + 64 * 24), c5);
    GROUP(4, c5, c5, c6, 0, 1, 1);
    c5 = far_taps<5>(sm, K, dch, ub, w2b, w3b, c5);
    GROUP(5, c5, c6, c7, 1, 1, 1);
    finish(5, c5);
    c6 = far_taps<6>(sm, K, dch, ub, w2b, w3b, c6);
    GROUP(6, c6, c7, c7, 1, 1, 0);
    finish(6, c6);
    c7 = far_taps<7>(sm, K, dch, ub, w2b, w3b, c7);
    GROUP(7, c7, c7, c7, 1, 0, 0);
    finish(7, c7);
  }
  STAMP(6);
  float y0 = 0.f, y1 = 0.f;
  if (w == 0) {
    u32x4 pp[7];
    for (int spin = 0; spin < SPIN_CAP; ++spin) {
      bool ok = true;
#pragma unroll
      for (int j = 0; j < 7; ++j) {
        pp[j] = __builtin_amdgcn_raw_buffer_load_b128(wr, WS_PART + grp * (NPROD * 64) + lane * 16 + j * 1024, 0, 16);
        ok &= (pp[j].y == tag) & (pp[j].w == tag);
      }
      if (__all(ok)) break;
      __builtin_amdgcn_s_sleep(1);
    }
#pragma unroll
    for (int j = 0; j < 7; ++j) { y0 += __uint_as_float(pp[j].x); y1 += __uint_as_float(pp[j].z); }
    y0 += dppf<ROR(4)>(y0); y1 += dppf<ROR(4)>(y1);
    y0 += dppf<ROR(8)>(y0); y1 += dppf<ROR(8)>(y1);
    y0 = sum32(sum16(y0)); y1 = sum32(sum16(y1));
    STAMP(7);
  }
  bar();
  if (w == 0 && lane < 4) {
    const u32 key = misc[12 + (lane >> 1)];
    const int row = (2048 - (key & 0xfff)) & 2047;
    u32 rb = key & 0xffff0000u;
    rb ^= ((int)rb < 0) ? 0x80000000u : 0xffff0000u;
    const float sg = rbf(__builtin_amdgcn_rcpf(1.f + __builtin_amdgcn_exp2f(__uint_as_float(rb) * -1.44269504f)));
    const u32 oa = XO_BASE + (lane & 1) * 8192 + row * 2;
    const float o0 = bflo(lds_ld<u16>(sm, oa)), o1 = bflo(lds_ld<u16>(sm, oa + 4096));
    *(u32*)(out + row * 512 + 4 * grp + 2 * (lane & 1)) = pkbf(o0 + rbf(rbf(y0) * sg), o1 + rbf(rbf(y1) * sg));
  }
  STAMP(8);
  STAMP_END(15);
}

extern "C" void kernel_launch(void* const* d_in, const int* in_sizes, int n_in, void* d_out, int out_size, void* d_ws,
                              size_t ws_size, hipStream_t stream) {
  fused_kernel<<<256, 512, 0, stream>>>(
      (const u16*)d_in[0], (const u16*)d_in[1], (const u16*)d_in[2], (const u16*)d_in[3], (const u16*)d_in[4],
      (const u16*)d_in[5], (const u16*)d_in[6], (const u16*)d_in[7], (const u16*)d_in[8], (const u16*)d_in[10],
      (const u16*)d_in[11], (const u16*)d_in[12], (const u16*)d_in[13], (u16*)d_out, (char*)d_ws);
}
```

```cpp
#include <hip/hip_runtime.h>
#include <stdint.h>

typedef unsigned short u16;
typedef unsigned int u32;
typedef unsigned long long u64;
typedef u32 u32x2 __attribute__((ext_vector_type(2)));
typedef u32 u32x4 __attribute__((ext_vector_type(4)));
typedef float f32x4 __attribute__((ext_vector_type(4)));
typedef float f32x2 __attribute__((ext_vector_type(2)));
typedef _Float16 h2 __attribute__((ext_vector_type(2)));
typedef _Float16 h8 __attribute__((ext_vector_type(8)));
typedef __bf16 b2 __attribute__((ext_vector_type(2)));
typedef __bf16 b8 __attribute__((ext_vector_type(8)));
typedef short s4 __attribute__((ext_vector_type(4)));
typedef u16 us2 __attribute__((ext_vector_type(2)));
#define LAS __attribute__((address_space(3)))
#define DEV __device__ __forceinline__

#define WS_CTRL 0
#define WS_STATS 4096
#define WS_KEYS 32768
#define WS_ACKS 65536
#define WS_PART 131072
#define WS_BYTES (2u << 20)
#define NPROD 112
#define SPIN_CAP 100000
#define CODE_LINES 0

#define TAB_RS 2080
#define KL_BASE 112320
#define KL_CS 1984
#define KL_CH (4 * KL_CS)
#define XO_BASE (KL_BASE + 4 * KL_CH)
#define MISC_BASE (XO_BASE + 16384)
#define LDS_BYTES (MISC_BASE + 256)
#define DCH 14944
#define U_OFF 480
#define W5_OFF 4616
#define W3_OFF (W5_OFF + 3104)
#define W9_OFF (W3_OFF + 3104)
#define W7_OFF (W9_OFF + 2080)
#define YO_BASE (4 * DCH)
#define KG0 592
#define KGS 52
#define KG4 (KG0 + 4 * KGS)
#define KG5 (KG4 + 84)

#define STAMP(i)
#define STAMP_DECL
#define STAMP_END(i)
#define SB() __builtin_amdgcn_sched_barrier(0)
#define PIN(v) asm volatile("" : "+v"(v))

DEV float bflo(u32 v) { return __uint_as_float(v << 16); }
DEV float bfhi(u32 v) { return __uint_as_float(v & 0xffff0000u); }
DEV u32 pkbf(float a, float b) { f32x2 f = {a, b}; return __builtin_bit_cast(u32, __builtin_convertvector(f, b2)); }
DEV float rbf(float a) { return bflo(pkbf(a, a)); }
DEV u32 pkh(float a, float b) { h2 h = {(_Float16)a, (_Float16)b}; return __builtin_bit_cast(u32, h); }
DEV u32 hadd(u32 a, u32 b) { return __builtin_bit_cast(u32, __builtin_bit_cast(h2, a) + __builtin_bit_cast(h2, b)); }
DEV f32x4 mma_h(u32x4 a, u32x4 b, f32x4 c) {
  return __builtin_amdgcn_mfma_f32_16x16x32_f16(__builtin_bit_cast(h8, a), __builtin_bit_cast(h8, b), c, 0, 0, 0);
}
DEV f32x4 mma_b(u32x4 a, u32x4 b, f32x4 c) {
  return __builtin_amdgcn_mfma_f32_16x16x32_bf16(__builtin_bit_cast(b8, a), __builtin_bit_cast(b8, b), c, 0, 0, 0);
}
template <int C> DEV u32 dppu(u32 v) { return __builtin_amdgcn_update_dpp(0, v, C, 0xf, 0xf, true); }
template <int C> DEV float dppf(float v) { return __uint_as_float(dppu<C>(__float_as_uint(v))); }
#define ROR(n) (0x120 + (n))
DEV float sum16(float v) { u32 u = __float_as_uint(v); auto r = __builtin_amdgcn_permlane16_swap(u, u, false, false); return __uint_as_float(r[0]) + __uint_as_float(r[1]); }
DEV float sum32(float v) { u32 u = __float_as_uint(v); auto r = __builtin_amdgcn_permlane32_swap(u, u, false, false); return __uint_as_float(r[0]) + __uint_as_float(r[1]); }
DEV u32 rowmax(u32 v) {
  v = max(v, dppu<ROR(1)>(v)); v = max(v, dppu<ROR(2)>(v)); v = max(v, dppu<ROR(4)>(v)); v = max(v, dppu<ROR(8)>(v));
  return v;
}
DEV u32 wavemax(u32 v) {
  v = rowmax(v);
  u32 a = __builtin_amdgcn_readlane(v, 0), b = __builtin_amdgcn_readlane(v, 16), c = __builtin_amdgcn_readlane(v, 32), d = __builtin_amdgcn_readlane(v, 48);
  return max(max(a, b), max(c, d));
}
DEV bool has_mark(u32 a, u32 b) {
  us2 m = __builtin_elementwise_max(__builtin_bit_cast(us2, a), __builtin_bit_cast(us2, b));
  return max(m.x, m.y) == 0xffff;
}
DEV void bar() { asm volatile("s_waitcnt lgkmcnt(0)\n\ts_barrier" ::: "memory"); }
DEV void bar0() { asm volatile("s_barrier" ::: "memory"); }

struct cf { float r, i; };
DEV cf cmul(cf a, cf b) { cf o; o.r = __builtin_fmaf(a.r, b.r, -(a.i * b.i)); o.i = __builtin_fmaf(a.r, b.i, a.i * b.r); return o; }
DEV cf csq(cf a) { cf o; o.r = __builtin_fmaf(a.r, a.r, -(a.i * a.i)); o.i = (a.r + a.r) * a.i; return o; }
DEV cf seed(float p2, float th, float tl, float v) {
  float e = __builtin_amdgcn_exp2f(p2 * v);
  float f = __builtin_fmaf(tl, v, __builtin_amdgcn_fractf(th * v));
  cf o; o.r = e * __builtin_amdgcn_cosf(f); o.i = e * __builtin_amdgcn_sinf(f); return o;
}
DEV h2 toh(cf a) { h2 h = {(_Float16)a.r, (_Float16)a.i}; return h; }
DEV h2 tohj(cf a) { h2 h = {(_Float16)a.r, (_Float16)-a.i}; return h; }
DEV h2 hmul(h2 a, h2 b) {
  h2 t = __builtin_shufflevector(a, a, 0, 0) * b;
  h2 bs = {-b.y, b.x};
  return __builtin_elementwise_fma(__builtin_shufflevector(a, a, 1, 1), bs, t);
}
DEV h2 hmulj(h2 a, h2 b) {
  h2 bc = {b.x, -b.y};
  h2 t = __builtin_shufflevector(a, a, 0, 0) * bc;
  h2 bs = {b.y, b.x};
  return __builtin_elementwise_fma(__builtin_shufflevector(a, a, 1, 1), bs, t);
}

template <typename T> DEV T lds_ld(LAS char* s, u32 off) { return *(LAS T*)(s + off); }
template <typename T> DEV void lds_st(LAS char* s, u32 off, T v) { *(LAS T*)(s + off) = v; }
DEV u32x2 lds_tr(LAS char* s, u32 off) { return __builtin_bit_cast(u32x2, __builtin_amdgcn_ds_read_tr16_b64_v4i16((LAS s4*)(s + off))); }
DEV u32x4 lds_tr2(LAS char* s, u32 o0, u32 o1) { u32x2 a = lds_tr(s, o0), b = lds_tr(s, o1); u32x4 r = {a.x, a.y, b.x, b.y}; return r; }

DEV void top2(__amdgpu_buffer_rsrc_t wr, u32 tag, int lane, u32x4 (&kk)[4], u32& K1, u32& K2) {
#pragma unroll 1
  for (int spin = 0; spin < SPIN_CAP; ++spin) {
    bool ok = true;
#pragma unroll
    for (int j = 0; j < 4; ++j) {
      kk[j] = __builtin_amdgcn_raw_buffer_load_b128(wr, WS_KEYS + lane * 16 + j * 1024, 0, 16);
      ok &= (kk[j].y == tag) & (kk[j].w == tag);
    }
    if (__all(ok)) break;
    __builtin_amdgcn_s_sleep(1);
  }
  K1 = wavemax(max(max(kk[0].x, kk[1].x), max(kk[2].x, kk[3].x)));
  u32 m = 0;
#pragma unroll
  for (int j = 0; j < 4; ++j) m = max(m, kk[j].x == K1 ? 0u : kk[j].x);
  K2 = wavemax(m);
}

DEV void kl_put(LAS char* sm, u32 a, u32 x, u32 y) {
  const u32 m = __builtin_amdgcn_alignbit(y, x, 16);
  lds_st<u32x2>(sm, a + 8, u32x2{x, y});
  lds_st<u32>(sm, a + 2 * KL_CS + 4, x);
  lds_st<u32>(sm, a + 2 * KL_CS + 8, y);
  lds_st<u16>(sm, a + KL_CS + 6, (u16)x);
  lds_st<u32>(sm, a + KL_CS + 8, m);
  lds_st<u16>(sm, a + KL_CS + 12, (u16)(y >> 16));
  lds_st<u16>(sm, a + 3 * KL_CS + 2, (u16)x);
  lds_st<u32>(sm, a + 3 * KL_CS + 4, m);
  lds_st<u16>(sm, a + 3 * KL_CS + 8, (u16)(y >> 16));
}

__global__ __launch_bounds__(512, 2) void fused_kernel(
    const u16* __restrict__ x, const u16* __restrict__ pAre, const u16* __restrict__ pAim, const u16* __restrict__ pBre,
    const u16* __restrict__ pBim, const u16* __restrict__ pCre, const u16* __restrict__ pCim, const u16* __restrict__ pLdt,
    const u16* __restrict__ pDs, const u16* __restrict__ pWr, const u16* __restrict__ pWg, const u16* __restrict__ pWu,
    const u16* __restrict__ pWd, u16* __restrict__ out, char* ws) {
  __shared__ __attribute__((aligned(16))) char smem[LDS_BYTES];
  LAS char* sm = (LAS char*)smem;
  const char* pc0 = (const char*)__builtin_amdgcn_s_getpc();
  const int tid = threadIdx.x, lane = tid & 63;
  const int w = __builtin_amdgcn_readfirstlane(tid >> 6);
  const int c = lane & 15, g = lane >> 4, q = (lane >> 2) & 3, p = lane & 3;
  const int b = blockIdx.x, xcd = b & 7, idx = b >> 3;
  const __amdgpu_buffer_rsrc_t wr = __builtin_amdgcn_make_buffer_rsrc(ws, 0, WS_BYTES, 0x00020000);
  const f32x4 z4 = {0.f, 0.f, 0.f, 0.f};
  STAMP_DECL;
  STAMP(0);

  if (b < 128) {
    const u32x4 ones = {0x3f803f80u, 0x3f803f80u, 0x3f803f80u, 0x3f803f80u};
    const bool prod = xcd < 4;
    const int rg = (idx * 4 + xcd) * 2 + (w >> 2);
    const int wq = w & 3;
    u32 tag, pf = 0;
    if (prod) {
      u32x4 xv[4], wv[4];
      const u16* xr = x + (16 * rg + c) * 512 + 128 * wq + 8 * g;
#pragma unroll
      for (int j = 0; j < 4; ++j) xv[j] = *(const u32x4*)(xr + 32 * j);
#pragma unroll
      for (int j = 0; j < 4; ++j) wv[j] = *(const u32x4*)(pWr + 128 * wq + 8 * g + 32 * j);
      const u32x4 wf = *(const u32x4*)(pWr + 8 * lane);
      tag = __builtin_amdgcn_raw_buffer_load_b32(wr, WS_CTRL, 0, 16);
      if (tid < CODE_LINES) pf = *(const u32*)(pc0 + tid * 128);
      SB();
      const bool selw = (c & 1) != 0;
      f32x4 d1 = z4, d2 = z4;
#pragma unroll
      for (int j = 0; j < 4; ++j) d2 = mma_b(xv[j], xv[j], d2);
#pragma unroll
      for (int j = 0; j < 4; ++j) d1 = mma_b(selw ? wv[j] : ones, xv[j], d1);
      f32x4 d3 = mma_b(ones, wf, z4);
      {
        float dg = p == 0 ? d2.x : p == 1 ? d2.y : p == 2 ? d2.z : d2.w;
        f32x4 pr = {d1.x, d1.y, dg, 0.f};
        if (g == q) lds_st<f32x4>(sm, w * 256 + c * 16, pr);
      }
      float sw = d3.x;
      sw += dppf<ROR(1)>(sw); sw += dppf<ROR(2)>(sw); sw += dppf<ROR(4)>(sw); sw += dppf<ROR(8)>(sw);
      tag += 1u;
      PIN(sw); PIN(tag);
      STAMP(1);
      bar();
      if (wq == 0) {
        f32x4 s = lds_ld<f32x4>(sm, w * 256 + lane * 16);
        s.x = sum32(sum16(s.x)); s.z = sum32(sum16(s.z)); s.y = sum32(sum16(s.y));
        float mean = s.x * (1.f / 512.f);
        float var = fmaxf(__builtin_fmaf(-mean, mean, s.z * (1.f / 512.f)), 0.f);
        float sd = __builtin_amdgcn_sqrtf(rbf(rbf(var) + 1e-5f));
        u32 ms = pkbf(mean, sd);
        u32 msn = dppu<0xb1>(ms);
        u32x4 gs = {ms, tag, msn, tag};
        if (lane < 16 && !(lane & 1)) __builtin_amdgcn_raw_buffer_store_b128(gs, wr, WS_STATS + rg * 128 + lane * 8, 0, 16);
        float r = __builtin_fmaf(-bflo(ms), sw, s.y) * __builtin_amdgcn_rcpf(bfhi(ms));
        u32 rb = pkbf(r, r) & 0xffff0000u;
        rb ^= ((int)rb < 0) ? 0xffff0000u : 0x80000000u;
        u32 key = rb | (u32)(2048 - (16 * rg + c));
        u32 k1 = rowmax(key);
        u32 k2 = rowmax(key == k1 ? 0u : key);
        u32x4 gk = {key, tag, ms, tag};
        if (lane < 16 && key >= k2) __builtin_amdgcn_raw_buffer_store_b128(gk, wr, WS_KEYS + rg * 32 + (key == k1 ? 0 : 16), 0, 16);
        STAMP(2);
      }
      bar0();
    } else {
      tag = __builtin_amdgcn_raw_buffer_load_b32(wr, WS_CTRL, 0, 16);
      if (tid < CODE_LINES) pf = *(const u32*)(pc0 + tid * 128);
    }
    if (idx >= 14) {
      if (!prod) tag += 1u;
      PIN(pf);
      if (tid == 0) __builtin_amdgcn_raw_buffer_store_b32(tag, wr, WS_ACKS + b * 4, 0, 16);
      if (b == 127 && w == 0) {
#pragma unroll 1
        for (int spin = 0; spin < SPIN_CAP; ++spin) {
          u32x4 a = __builtin_amdgcn_raw_buffer_load_b128(wr, WS_ACKS + lane * 16, 0, 16);
          if (__all(a.x == tag && a.y == tag && a.z == tag && a.w == tag)) break;
          __builtin_amdgcn_s_sleep(8);
        }
        if (lane == 0) __builtin_amdgcn_raw_buffer_store_b32(tag, wr, WS_CTRL, 0, 16);
      }
      STAMP_END(15);
      return;
    }
    const int fi = xcd * 14 + idx;
    u32x4 wa[4], wb[2];
    {
      const u16* src = (w & 4 ? pWu : pWg) + (128 * wq + (lane >> 1)) * 1792 + 16 * fi + 8 * (lane & 1);
#pragma unroll
      for (int j = 0; j < 4; ++j) wa[j] = *(const u32x4*)(src + j * 32 * 1792);
      const u16* sd = pWd + (16 * fi + (lane >> 3)) * 512 + 64 * w + 8 * (lane & 7);
#pragma unroll
      for (int j = 0; j < 2; ++j) wb[j] = *(const u32x4*)(sd + j * 8 * 512);
    }
    SB();
    if (!prod) tag += 1u;
    PIN(pf);
    if (tid == 0) __builtin_amdgcn_raw_buffer_store_b32(tag, wr, WS_ACKS + b * 4, 0, 16);
    const u32 wbase = 8192 + w * 6144;
#pragma unroll
    for (int j = 0; j < 4; ++j) lds_st<u32x4>(sm, wbase + j * 1024 + lane * 16, wa[j]);
#pragma unroll
    for (int j = 0; j < 2; ++j) lds_st<u32x4>(sm, wbase + 4096 + j * 1024 + lane * 16, wb[j]);
    u32x4 fa[4], fb[2];
    f32x4 ds = z4;
#pragma unroll
    for (int j = 0; j < 4; ++j) {
      fa[j] = lds_tr2(sm, wbase + j * 1024 + (8 * g + q) * 32 + 8 * p, wbase + j * 1024 + (8 * g + q) * 32 + 8 * p + 128);
      ds = mma_b(fa[j], ones, ds);
    }
#pragma unroll
    for (int j = 0; j < 2; ++j)
      fb[j] = lds_tr2(sm, wbase + 4096 + (4 * g + q) * 128 + j * 64 + 8 * p, wbase + 4096 + (4 * g + q) * 128 + j * 64 + 32 + 8 * p);
    PIN(ds); PIN(fb[0]); PIN(fb[1]);
    STAMP(3);
    if (w == 0) {
      u32x4 kk[4]; u32 K1, K2;
      top2(wr, tag, lane, kk, K1, K2);
#pragma unroll
      for (int j = 0; j < 4; ++j)
        if (kk[j].x >= K2) { u32x2 v = {kk[j].x, kk[j].z}; lds_st<u32x2>(sm, 4096 + (kk[j].x == K1 ? 0 : 8), v); }
    }
    bar();
    STAMP(4);
    const u32x2 sel = lds_ld<u32x2>(sm, 4096 + (q & 1) * 8);
    {
      const int row = (2048 - (sel.x & 0xfff)) & 2047;
      const u16* xr = x + row * 512 + 128 * wq + 8 * g;
      u32x4 xv[4];
#pragma unroll
      for (int j = 0; j < 4; ++j) xv[j] = *(const u32x4*)(xr + 32 * j);
      f32x4 d = ds * -bflo(sel.y);
#pragma unroll
      for (int j = 0; j < 4; ++j) d = mma_b(fa[j], xv[j], d);
      const float dg = p == 0 ? d.x : p == 1 ? d.y : p == 2 ? d.z : d.w;
      if (c < 8) lds_st<float>(sm, (((w >> 2) * 2 + q) * 16 + 4 * g + p) * 16 + 4 * wq, dg);
    }
    const float rs = __builtin_amdgcn_rcpf(bfhi(sel.y));
    bar();
    STAMP(5);
    {
      const f32x4 s = lds_ld<f32x4>(sm, (q * 16 + 4 * g + p) * 16);
      const float v = rbf(((s.x + s.y) + (s.z + s.w)) * rs);
      const float o = dppf<ROR(8)>(v);
      const float ga = (c & 8) ? o : v, up = (c & 8) ? v : o;
      const float sl = ga * __builtin_amdgcn_rcpf(1.f + __builtin_amdgcn_exp2f(ga * -1.44269504f));
      const float hv = rbf(sl) * up;
      const u32 hp = pkbf(hv, dppf<0xb1>(hv));
      const u32 h0 = dppu<0x00>(hp), h1 = dppu<0xaa>(hp);
      const u32x4 b0 = {p == 0 ? h0 : 0u, p == 0 ? h1 : 0u, p == 1 ? h0 : 0u, p == 1 ? h1 : 0u};
      const u32x4 b1 = {p == 2 ? h0 : 0u, p == 2 ? h1 : 0u, p == 3 ? h0 : 0u, p == 3 ? h1 : 0u};
      const f32x4 d = mma_b(fb[1], b1, mma_b(fb[0], b0, z4));
      u32x4 gp = {__float_as_uint((c & 8) ? d.z : d.x), tag, __float_as_uint((c & 8) ? d.w : d.y), tag};
      const u32 grp = 16 * w + 4 * p + g;
      __builtin_amdgcn_raw_buffer_store_b128(gp, wr, WS_PART + (grp * NPROD + fi) * 64 + (q & 1) * 32 + (c >> 3) * 16, 0, 16);
    }
    STAMP(6);
    STAMP_END(15);
    return;
  }

  const int grp = xcd * 16 + idx - 16;
  const int ch = w & 3, hb = w >> 2;
  const int chan = 4 * grp + ch;
  const u32 vld = pLdt[chan];
  const u32 vAre = *(const u32*)(pAre + chan * 128 + 2 * lane), vAim = *(const u32*)(pAim + chan * 128 + 2 * lane);
  const u32 vBre = *(const u32*)(pBre + chan * 128 + 2 * lane), vBim = *(const u32*)(pBim + chan * 128 + 2 * lane);
  const u32 vCre = *(const u32*)(pCre + chan * 128 + 2 * lane), vCim = *(const u32*)(pCim + chan * 128 + 2 * lane);
  const u32 vds = pDs[chan];
  u32 tag = __builtin_amdgcn_raw_buffer_load_b32(wr, WS_CTRL, 0, 16);
  u32 pf = 0;
  if (tid < CODE_LINES) pf = *(const u32*)(pc0 + tid * 128);
  else if (tid >= 384) pf = *(const u32*)(x + ((idx - 16) * 128 + tid - 384) * 512 + xcd * 64);
  bar0();
  u32x2 xc[4];
#pragma unroll
  for (int i = 0; i < 4; ++i) xc[i] = *(const u32x2*)(x + (4 * tid + i) * 512 + 4 * grp);
  SB();
  const u32x4 zz = {0u, 0u, 0u, 0u};
#pragma unroll
  for (int i = 0; i < 4; ++i) if (i < 3 || w < 7) lds_st<u32x4>(sm, KL_BASE + i * 8192 + tid * 16, zz);
  if (tid < 16) lds_st<u32x4>(sm, MISC_BASE + tid * 16, zz);
  const u32 tb = ch * 512 + hb * 256;
  u32 qa = tb + g * 16 + c * TAB_RS;
  u32 pa = tb + g * 16 + (32 + (u32)((0xED444A9987653210ull >> (c * 4)) & 15)) * TAB_RS;
  u32 pb = tb + g * 16 + (43 + (u32)((0x6660666A98765410ull >> (c * 4)) & 15)) * TAB_RS;
  const u32 kl = KL_BASE + ch * KL_CH;
  u32 da_dst = kl - 8 + 2 * (16 + (g >> 1) * 256 + 16 * c + 8 * hb + 4 * (g & 1));
  u32 db_dst, selx = 0x03020100u, sely = 0x07060504u;
  {
    int dst = c < 2 ? KG0 + hb * KGS + 16 * c : c < 4 ? KG0 + (3 - hb) * KGS + 16 * (c - 2) : (hb ? KG5 : KG4) + 16 * (c - 4);
    dst += 4 * g;
    if (hb == 0 && g == 0) {
      if (c == 0 || c == 4) selx = 0x03020c0cu;
      if (c >= 8) { selx = 0x0c0c0100u; sely = 0x0c0c0c0cu; }
      if (c == 9) dst = KG0 + 32;
      if (c == 10) dst = 572;
      if (c == 11) { dst = 568; selx = 0x0c0c0c0cu; sely = 0x01000c0cu; }
      if (c == 12) { dst = 528; selx = 0x01000100u; sely = 0x0c0c0100u; }
    }
    db_dst = kl - 8 + 2 * dst;
  }
  const bool db_en = c < 8 || (hb == 0 && g == 0 && c <= 12);
  PIN(qa); PIN(pa); PIN(pb); PIN(da_dst); PIN(db_dst); PIN(selx); PIN(sely);
  asm volatile("s_mov_b32 m0, %0\n\ts_nop 0" :: "s"(tb));
  SB();
  float inv;
  f32x4 ka, kb;
  {
#define WT(row, v) asm volatile("ds_write_addtid_b32 %0 offset:%1" :: "v"(v), "n"((row) * TAB_RS) : "memory")
#define WP(slot, v) WT(8 + (slot), v)
    const float dt = rbf(__builtin_amdgcn_exp2f(bflo(vld) * 1.44269504f));
    const float are = hb ? bfhi(vAre) : bflo(vAre), aim = hb ? bfhi(vAim) : bflo(vAim);
    const float p2 = (dt * are) * 1.44269504f;
    const float qq = dt * aim;
    const float th = qq * 0.15915494f;
    const float tl = __builtin_fmaf(qq, 6.4206382e-9f, __builtin_fmaf(qq, 0.15915494f, -th));
    STAMP(9);
    cf E16 = seed(p2, th, tl, 16.f), E1 = seed(p2, th, tl, 1.f), E256 = seed(p2, th, tl, 256.f);
    cf E32 = csq(E16), E64 = csq(E32), E128 = csq(E64), E2 = csq(E1), E4 = csq(E2), E8 = csq(E4), E512 = csq(E256), E1k = csq(E512);
    const float d1 = 1.f + bflo(vds);
    const float dl = dt * (1.44269504f * 2048.f);
    float b0 = (fabsf(bflo(vCre)) + fabsf(bflo(vCim))) * (fabsf(bflo(vBre)) + fabsf(bflo(vBim))) * __builtin_amdgcn_exp2f(fmaxf(bflo(vAre), 0.f) * dl);
    float b1 = (fabsf(bfhi(vCre)) + fabsf(bfhi(vCim))) * (fabsf(bfhi(vBre)) + fabsf(bfhi(vBim))) * __builtin_amdgcn_exp2f(fmaxf(bfhi(vAre), 0.f) * dl);
    float bd = fmaxf(fmaxf(b0, b1) * dt, fabsf(d1) * (1.f / 128.f));
    int be = (int)(wavemax(__float_as_uint(bd)) >> 23);
    int se = min(max(260 - be, 1), 253);
    const float sc = __uint_as_float((u32)se << 23);
    inv = __uint_as_float((u32)(254 - se) << 23);
    h2 Q[16];
    const h2 z8 = tohj(E8);
    Q[0] = h2{(_Float16)1.f, (_Float16)0.f}; Q[1] = tohj(E16); Q[2] = tohj(E32); Q[4] = tohj(E64); Q[8] = tohj(E128);
    Q[3] = hmul(Q[1], Q[2]);
#pragma unroll
    for (int i = 5; i < 8; ++i) Q[i] = hmul(Q[i - 4], Q[4]);
#pragma unroll
    for (int i = 9; i < 16; ++i) Q[i] = hmul(Q[i - 8], Q[8]);
#pragma unroll
    for (int i = 0; i < 16; ++i) WT(i, Q[i]);
    u32x4 fq0[4], fq1[4], fa[4], fb[4];
#pragma unroll
    for (int k = 0; k < 4; ++k) fq0[k] = lds_ld<u32x4>(sm, qa + k * 64);
    WT(16, z8);
#pragma unroll
    for (int i = 1; i < 16; ++i) { h2 t = hmul(Q[i], z8); WT(16 + i, t); }
#pragma unroll
    for (int k = 0; k < 4; ++k) fq1[k] = lds_ld<u32x4>(sm, qa + 16 * TAB_RS + k * 64);
    asm volatile("s_mov_b32 m0, %0\n\ts_nop 0" :: "s"(tb + 24 * TAB_RS));
    cf B = {(hb ? bfhi(vBre) : bflo(vBre)) * dt, (hb ? bfhi(vBim) : bflo(vBim)) * dt};
    cf C = {(hb ? bfhi(vCre) : bflo(vCre)) * sc, (hb ? bfhi(vCim) : bflo(vCim)) * sc};
    cf P0 = cmul(C, B), R0 = cmul(P0, E256);
    const h2 z1 = toh(E1), z2 = toh(E2), z4h = toh(E4), p0 = toh(P0), r0 = toh(R0);
    h2 p1 = hmul(p0, z1), p2h = hmul(p0, z2), p3 = hmul(p1, z2), r2 = hmul(r0, z2);
    WP(0, p0); WP(1, p1); WP(2, p2h); WP(3, p3);
    { h2 t = hmul(p0, z4h); WP(5, t); } { h2 t = hmul(p1, z4h); WP(6, t); } { h2 t = hmul(p2h, z4h); WP(7, t); } { h2 t = hmul(p3, z4h); WP(8, t); }
    WP(9, r0); WP(10, r2);
    { h2 t = hmul(r0, z4h); WP(4, t); } { h2 t = hmul(r2, z4h); WP(13, t); } { h2 t = hmulj(r0, z8); WP(14, t); }
#pragma unroll
    for (int k = 0; k < 4; ++k) fa[k] = lds_ld<u32x4>(sm, pa + k * 64);
    cf S0 = cmul(P0, E512), S1 = cmul(R0, E512), T0 = cmul(P0, E1k);
    const h2 s0 = toh(S0), s1 = toh(S1);
    WP(11, s0); WP(12, s1);
    { h2 t = hmul(s0, z4h); WP(15, t); } { h2 t = hmul(s1, z4h); WP(16, t); }
    { h2 t = toh(T0); WP(17, t); } { h2 t = toh(cmul(R0, E1k)); WP(18, t); } { h2 t = toh(cmul(S0, E1k)); WP(19, t); }
    { h2 t = toh(cmul(S1, E1k)); WP(20, t); } { h2 t = toh(cmul(T0, E1k)); WP(21, t); }
#pragma unroll
    for (int k = 0; k < 4; ++k) fb[k] = lds_ld<u32x4>(sm, pb + k * 64);
    STAMP(10);
    f32x4 a0 = z4, a1 = z4, e0 = z4, e1 = z4;
    if (lane == 0 && hb == 0) a0.x = d1 * sc;
#pragma unroll
    for (int k = 0; k < 4; ++k) { a0 = mma_h(fa[k], fq0[k], a0); a1 = mma_h(fa[k], fq1[k], a1); }
#pragma unroll
    for (int k = 0; k < 4; ++k) { e0 = mma_h(fq0[k], fb[k], e0); e1 = mma_h(fq1[k], fb[k], e1); }
    lds_st<f32x4>(sm, XO_BASE + w * 2048 + lane * 16, hb ? a0 : a1);
    lds_st<f32x4>(sm, XO_BASE + w * 2048 + 1024 + lane * 16, hb ? e0 : e1);
    ka = hb ? a1 : a0; kb = hb ? e1 : e0;
  }
  STAMP(1);
  bar();
  {
    ka += lds_ld<f32x4>(sm, XO_BASE + (w ^ 4) * 2048 + lane * 16);
    kb += lds_ld<f32x4>(sm, XO_BASE + (w ^ 4) * 2048 + 1024 + lane * 16);
    const u32x4 ff = {~0u, ~0u, ~0u, ~0u};
    lds_st<u32x4>(sm, YO_BASE + tid * 16, ff);
    lds_st<u32x4>(sm, YO_BASE + 8192 + tid * 16, ff);
    if (hb == 0) { if (lane < 30) lds_st<u32x4>(sm, ch * DCH + lane * 16, zz); }
    else if (lane < 8) lds_st<u32x4>(sm, ch * DCH + W5_OFF - 40 + (lane & 1) * 16 + ((lane >> 1) == 3 ? 2 * 3104 + 2080 : (lane >> 1) * 3104), zz);
    else if (lane < 24) lds_st<u32x4>(sm, YO_BASE + 16384 + ch * 256 + (lane - 8) * 16, ff);
    kl_put(sm, da_dst, pkh(ka.x, ka.y), pkh(ka.z, ka.w));
    const u32 bx = pkh(kb.x, kb.y), by = pkh(kb.z, kb.w);
    if (db_en) kl_put(sm, db_dst, __builtin_amdgcn_perm(by, bx, selx), __builtin_amdgcn_perm(by, bx, sely));
  }
  u32 aa, a5, a3, at;
  {
    const u32 k8 = kl + 8 * p;
    const int io = 32 - 8 * g - q - 4, sg = io & 3;
    aa = k8 + sg * KL_CS + 2 * (io - sg);
    const int kap = 16 * (g >> 1) + 4 * (g & 1) + q + 8;
    const int n0 = 130 - 64 - kap, par = n0 & 1, a0 = n0 >> 1;
    const int i5 = KG0 + par * KGS + a0 - 32, i3 = par ? KG0 + 3 * KGS + a0 - 32 : KG0 + 2 * KGS + a0 - 33;
    a5 = k8 + (i5 & 3) * KL_CS + 2 * (i5 & ~3); a3 = k8 + (i3 & 3) * KL_CS + 2 * (i3 & ~3);
    const int n1 = 65 - (8 + 4 * (g & 1) + q);
    const int it = g < 2 ? KG4 + n1 - 64 : KG5 + n1 - 65;
    at = k8 + (it & 3) * KL_CS + 2 * (it & ~3);
    PIN(aa); PIN(a5); PIN(a3); PIN(at);
  }
  bar();
  STAMP(2);
  u32x4 s0 = __builtin_amdgcn_raw_buffer_load_b128(wr, WS_STATS + tid * 32, 0, 16);
  u32x4 s1 = __builtin_amdgcn_raw_buffer_load_b128(wr, WS_STATS + tid * 32 + 16, 0, 16);
  u32x4 Ka[18], K5[3], K3[3], Kt[4];
#pragma unroll
  for (int t = 0; t < 18; ++t) Ka[t] = lds_tr2(sm, aa + 64 * t + 8, aa + 64 * t);
#pragma unroll
  for (int i = 0; i < 3; ++i) {
    K5[i] = lds_tr2(sm, a5 + 32 * (2 - i) + 8, a5 + 32 * (2 - i));
    K3[i] = lds_tr2(sm, a3 + 32 * (2 - i) + 8, a3 + 32 * (2 - i));
  }
#pragma unroll
  for (int i = 0; i < 4; ++i) Kt[i] = lds_tr2(sm, at + 32 * i + 16, at + 32 * i);
  SB();
  tag += 1u;
  PIN(pf);
  if (tid == 0) __builtin_amdgcn_raw_buffer_store_b32(tag, wr, WS_ACKS + b * 4, 0, 16);
  float xf[4][4];
  {
    u32x2 xt[4];
    xt[0] = u32x2{__builtin_amdgcn_perm(xc[1].x, xc[0].x, 0x05040100), __builtin_amdgcn_perm(xc[3].x, xc[2].x, 0x05040100)};
    xt[1] = u32x2{__builtin_amdgcn_perm(xc[1].x, xc[0].x, 0x07060302), __builtin_amdgcn_perm(xc[3].x, xc[2].x, 0x07060302)};
    xt[2] = u32x2{__builtin_amdgcn_perm(xc[1].y, xc[0].y, 0x05040100), __builtin_amdgcn_perm(xc[3].y, xc[2].y, 0x05040100)};
    xt[3] = u32x2{__builtin_amdgcn_perm(xc[1].y, xc[0].y, 0x07060302), __builtin_amdgcn_perm(xc[3].y, xc[2].y, 0x07060302)};
#pragma unroll
    for (int i = 0; i < 4; ++i) lds_st<u32x2>(sm, XO_BASE + i * 4096 + tid * 8, xt[i]);
#pragma unroll
    for (int i = 0; i < 4; ++i) {
      xf[0][i] = bflo(xc[i].x); xf[1][i] = bfhi(xc[i].x); xf[2][i] = bflo(xc[i].y); xf[3][i] = bfhi(xc[i].y);
      PIN(xf[0][i]); PIN(xf[1][i]); PIN(xf[2][i]); PIN(xf[3][i]);
    }
  }
  const u32 dch = ch * DCH;
  u32 ub = dch + U_OFF - 32 + 32 * c + 16 * g - 448;
  u32 w2b = dch + W5_OFF - 32 + (16 * (g >> 1) + 4 * (g & 1) + q) * 16 + 8 * p;
  u32 w3b = dch + (g < 2 ? W9_OFF : W7_OFF + 16) - 32 + (4 * (g & 1) + q) * 32 + 8 * p;
  u32 xo = XO_BASE + ch * 4096 + 32 * c + 8 * g;
  u32 yw = YO_BASE + 136 * c + 32 * g + 2 * ch;
  u32 yr = YO_BASE + 544 * ch + 8 * lane + 8 * g;
  u32 go = (64 * ch + lane) * 1024 + grp * 8;
  PIN(ub); PIN(w2b); PIN(w3b); PIN(xo); PIN(yw); PIN(yr); PIN(go);
  STAMP(3);
  {
#pragma unroll 1
    for (int spin = 0; spin < SPIN_CAP; ++spin) {
      const bool ok = (s0.y == tag) & (s0.w == tag) & (s1.y == tag) & (s1.w == tag);
      if (__all(ok)) break;
      if (!ok) {
        s0 = __builtin_amdgcn_raw_buffer_load_b128(wr, WS_STATS + tid * 32, 0, 16);
        s1 = __builtin_amdgcn_raw_buffer_load_b128(wr, WS_STATS + tid * 32 + 16, 0, 16);
      }
    }
    STAMP(4);
    const float r0 = __builtin_amdgcn_rcpf(bfhi(s0.x)), r1 = __builtin_amdgcn_rcpf(bfhi(s0.z));
    const float r2 = __builtin_amdgcn_rcpf(bfhi(s1.x)), r3 = __builtin_amdgcn_rcpf(bfhi(s1.z));
    const float n0 = -bflo(s0.x) * r0, n1 = -bflo(s0.z) * r1, n2 = -bflo(s1.x) * r2, n3 = -bflo(s1.z) * r3;
#pragma unroll
    for (int i = 0; i < 4; ++i) {
      u32x2 u = {pkh(__builtin_fmaf(xf[i][0], r0, n0), __builtin_fmaf(xf[i][1], r1, n1)),
                 pkh(__builtin_fmaf(xf[i][2], r2, n2), __builtin_fmaf(xf[i][3], r3, n3))};
      lds_st<u32x2>(sm, i * DCH + U_OFF + tid * 8, u);
    }
  }
  bar();
  STAMP(5);
  volatile LAS u32* misc = (volatile LAS u32*)(sm + MISC_BASE);
  char* const outb = (char*)out;

#define LDU(j, F) _Pragma("unroll") for (int e = 0; e < 8; ++e) F[e] = lds_ld<u32x4>(sm, ub + 512 * (j) + 64 * e)
#define LDW(u, A5, A3) A5 = lds_tr2(sm, w2b + 512 * (u), w2b + 512 * (u) + 128); A3 = lds_tr2(sm, w2b + 3112 + 512 * (u), w2b + 3112 + 512 * (u) + 128)
#define LDT(u) lds_tr2(sm, w3b + 512 * (u), w3b + 512 * (u) + 256)
#define LDS17(T) lds_ld<u32x4>(sm, ub + 448 + 2 * (256 * (T) - 1032 + 16))
#define LDX(T) lds_ld<u32x2>(sm, xo + 512 * (T))
#define EPI(T, acc, xv) do {                                                                                   \
    const u32 o0_ = pkbf(__builtin_fmaf(acc.x, inv, bflo(xv.x)), __builtin_fmaf(acc.y, inv, bfhi(xv.x)));       \
    const u32 o1_ = pkbf(__builtin_fmaf(acc.z, inv, bflo(xv.y)), __builtin_fmaf(acc.w, inv, bfhi(xv.y)));       \
    lds_st<u16>(sm, yw + 2176 * (T), (u16)o0_); lds_st<u16>(sm, yw + 2176 * (T) + 8, (u16)(o0_ >> 16));         \
    lds_st<u16>(sm, yw + 2176 * (T) + 16, (u16)o1_); lds_st<u16>(sm, yw + 2176 * (T) + 24, (u16)(o1_ >> 16));   \
  } while (0)
#define LDY(T) (*(volatile LAS u32x2*)(sm + yr + 2176 * (T)))
#define STO(T, yv) do {                                                                                        \
    _Pragma("unroll 1") for (int spin = 0; spin < SPIN_CAP * 16; ++spin) {                                     \
      if (!__any(has_mark(yv.x, yv.y))) break;                                                                 \
      yv = LDY(T);                                                                                             \
    }                                                                                                          \
    *(u32x2*)(outb + (T) * 262144 + go) = yv;                                                                  \
  } while (0)
#define NEAR(F, e0, e1, A0, A1, D0, D1) _Pragma("unroll") for (int e = e0; e < e1; ++e) {                       \
    if (D0) A0 = mma_h(Ka[7 - e], F[e], A0);                                                                   \
    if (D1) A1 = mma_h(Ka[15 - e], F[e], A1); }
#define MIX(n) _Pragma("unroll") for (int i_ = 0; i_ < n; ++i_) {                                              \
    __builtin_amdgcn_sched_group_barrier(0x008, 1, 0); __builtin_amdgcn_sched_group_barrier(0x080, 1, 0);      \
    __builtin_amdgcn_sched_group_barrier(0x002, 2, 0); }

  u32x4 F0[8], F1[8], A5, A3, B5, B3, C5;
  u32x2 xv, yv;
  if (hb == 0) {
    f32x4 c0 = z4, c1 = z4, c2, c3, c4;
    LDU(0, F0);
    SB();
    LDU(1, F1); xv = LDX(0);
    NEAR(F0, 0, 8, c0, c1, 1, 1); c2 = mma_h(Ka[16], F0[7], z4);
    MIX(17); SB();
    NEAR(F1, 0, 4, c1, c2, 1, 1);
    EPI(0, c0, xv); xv = LDX(1);
    MIX(8); SB();
    LDU(2, F0); yv = LDY(0);
    NEAR(F1, 4, 8, c1, c2, 1, 1); c3 = mma_h(Ka[16], F1[7], z4);
    MIX(9); SB();
    STO(0, yv);
    NEAR(F0, 0, 4, c2, c3, 1, 1);
    EPI(1, c1, xv); xv = LDX(2);
    MIX(8); SB();
    LDU(3, F1); yv = LDY(1);
    u32 wf = misc[ch];
    NEAR(F0, 4, 8, c2, c3, 1, 1); c4 = mma_h(Ka[16], F0[7], z4);
    MIX(9); SB();
    STO(1, yv);
#pragma unroll 1
    for (int spin = 0; wf == 0u && spin < SPIN_CAP * 16; ++spin) wf = misc[ch];
    asm volatile("" ::: "memory");
    LDW(0, A5, A3); LDW(1, B5, B3);
    NEAR(F1, 0, 4, c3, c4, 1, 1);
    MIX(8); SB();
    c2 = mma_h(A5, K5[2], c2); c3 = mma_h(A5, K5[1], c3); c2 = mma_h(A3, K3[2], c2); c3 = mma_h(A3, K3[1], c3);
    c4 = mma_h(A5, K5[0], c4); c3 = mma_h(B5, K5[2], c3); c4 = mma_h(A3, K3[0], c4); c3 = mma_h(B3, K3[2], c3);
    LDU(4, F0);
    MIX(8); SB();
    NEAR(F1, 4, 8, c3, c4, 1, 1);
    EPI(2, c2, xv); xv = LDX(3);
    MIX(8); SB();
    yv = LDY(2);
    LDW(2, A5, A3); C5 = LDT(0); F1[0] = LDS17(4);
    c4 = mma_h(B5, K5[1], c4); c4 = mma_h(B3, K3[1], c4);
    NEAR(F0, 0, 4, c4, c4, 1, 0);
    MIX(6); SB();
    STO(2, yv);
    EPI(3, c3, xv); xv = LDX(4);
    NEAR(F0, 4, 8, c4, c4, 1, 0);
    c4 = mma_h(A5, K5[2], c4); c4 = mma_h(A3, K3[2], c4);
    MIX(6); SB();
    yv = LDY(3);
    c4 = mma_h(C5, Kt[0], c4); c4 = mma_h(Ka[17], F1[0], c4);
    SB();
    STO(3, yv);
    EPI(4, c4, xv);
    yv = LDY(4);
    STO(4, yv);
    if (ch == 0) {
      u32x4 kk[4]; u32 K1, K2;
      top2(wr, tag, lane, kk, K1, K2);
      if (lane == 0) { misc[4] = K1; misc[5] = K2; }
    }
  } else {
    {
      u32x4 ra[3], rb[3];
      const u32 o = dch + lane * 16;
#pragma unroll
      for (int it = 0; it < 3; ++it) { ra[it] = lds_ld<u32x4>(sm, o + U_OFF - 16 + 1024 * it); rb[it] = lds_ld<u32x4>(sm, o + U_OFF + 1024 * it); }
      SB();
#pragma unroll
      for (int it = 0; it < 3; ++it) {
        const u32 r[8] = {ra[it].x, ra[it].y, ra[it].z, ra[it].w, rb[it].x, rb[it].y, rb[it].z, rb[it].w};
        u32 od[7];
#pragma unroll
        for (int i = 0; i < 7; ++i) od[i] = __builtin_amdgcn_alignbit(r[i + 1], r[i], 16);
        u32x4 w3, w5, w7, w9;
#pragma unroll
        for (int j = 2; j < 6; ++j) {
          w3[j - 2] = hadd(hadd(od[j - 1], od[j]), r[j]);
          w5[j - 2] = hadd(hadd(r[j - 1], r[j + 1]), w3[j - 2]);
        }
        lds_st<u32x4>(sm, o + W5_OFF - 8 + 1024 * it, w5);
        lds_st<u32x4>(sm, o + W3_OFF - 8 + 1024 * it, w3);
        if (it < 2) {
#pragma unroll
          for (int j = 2; j < 6; ++j) {
            w7[j - 2] = hadd(hadd(od[j - 2], od[j + 1]), w5[j - 2]);
            w9[j - 2] = hadd(hadd(r[j - 2], r[j + 2]), w7[j - 2]);
          }
          lds_st<u32x4>(sm, o + W9_OFF - 8 + 1024 * it, w9);
          lds_st<u32x4>(sm, o + W7_OFF - 8 + 1024 * it, w7);
        }
        if (it == 1) { LDU(4, F0); C5 = lds_ld<u32x4>(sm, ub + 64 * 31); }
      }
      asm volatile("" ::: "memory");
      if (lane == 0) misc[ch] = 1u;
      asm volatile("" ::: "memory");
    }
    SB();
    STAMP(11);
    f32x4 c5, c6, c7;
    LDU(5, F1); xv = LDX(5);
    c5 = mma_h(Ka[16], C5, z4);
    NEAR(F0, 0, 8, c5, c5, 0, 1); c6 = mma_h(Ka[16], F0[7], z4);
    MIX(10); SB();
    LDW(1, A5, A3); LDW(2, B5, B3); LDW(3, F0[0], F0[1]); F0[2] = LDT(0); F0[3] = LDT(1); F0[4] = LDS17(5);
    NEAR(F1, 0, 8, c5, c6, 1, 1); c7 = mma_h(Ka[16], F1[7], z4);
    MIX(17); SB();
    LDU(6, F1);
    c5 = mma_h(A5, K5[0], c5); c6 = mma_h(B5, K5[0], c6); c5 = mma_h(A3, K3[0], c5); c6 = mma_h(B3, K3[0], c6);
    c5 = mma_h(B5, K5[1], c5); c6 = mma_h(F0[0], K5[1], c6); c5 = mma_h(B3, K3[1], c5); c6 = mma_h(F0[1], K3[1], c6);
    c5 = mma_h(F0[0], K5[2], c5); c6 = mma_h(F0[2], Kt[2], c6); c5 = mma_h(F0[1], K3[2], c5); c6 = mma_h(F0[3], Kt[1], c6);
    c5 = mma_h(F0[2], Kt[1], c5); c5 = mma_h(F0[3], Kt[0], c5); c5 = mma_h(Ka[17], F0[4], c5);
    MIX(15); SB();
    c7 = mma_h(F0[0], K5[0], c7); c7 = mma_h(F0[1], K3[0], c7); c7 = mma_h(F0[2], Kt[3], c7); c7 = mma_h(F0[3], Kt[2], c7);
    NEAR(F1, 0, 3, c6, c7, 1, 1);
    EPI(5, c5, xv); xv = LDX(6);
    LDW(4, A5, A3);
    MIX(10); SB();
    yv = LDY(5);
    B5 = LDT(2); B3 = LDS17(6);
    LDU(7, F0);
    NEAR(F1, 3, 8, c6, c7, 1, 1);
    c6 = mma_h(A5, K5[2], c6); c6 = mma_h(A3, K3[2], c6);
    MIX(12); SB();
    STO(5, yv);
    c6 = mma_h(B5, Kt[0], c6); c6 = mma_h(Ka[17], B3, c6);
    c7 = mma_h(A5, K5[1], c7); c7 = mma_h(A3, K3[1], c7); c7 = mma_h(B5, Kt[1], c7);
    LDW(5, F1[0], F1[1]); F1[2] = LDT(3); F1[3] = LDS17(7);
    MIX(5); SB();
    NEAR(F0, 0, 8, c7, c7, 1, 0);
    EPI(6, c6, xv); xv = LDX(7);
    MIX(8); SB();
    yv = LDY(6);
    c7 = mma_h(F1[0], K5[2], c7); c7 = mma_h(F1[1], K3[2], c7); c7 = mma_h(F1[2], Kt[0], c7); c7 = mma_h(Ka[17], F1[3], c7);
    SB();
    STO(6, yv);
    EPI(7, c7, xv);
    yv = LDY(7);
    STO(7, yv);
  }
  STAMP(6);
  {
    u32 k1 = 0, k2 = 0;
#pragma unroll 1
    for (int spin = 0; (k1 == 0u || k2 == 0u) && spin < SPIN_CAP * 64; ++spin) { k1 = misc[4]; k2 = misc[5]; }
    const u32 key = (lane & 2) ? k2 : k1;
    const int row = (2048 - (key & 0xfff)) & 2047;
    const bool own = ((row >> 6) & 3) == ch && (row >= 1280) == (hb != 0);
    if (__any(own)) {
      u32x4 pp[7];
#pragma unroll 1
      for (int spin = 0; spin < SPIN_CAP; ++spin) {
        bool ok = true;
#pragma unroll
        for (int j = 0; j < 7; ++j) {
          pp[j] = __builtin_amdgcn_raw_buffer_load_b128(wr, WS_PART + grp * (NPROD * 64) + lane * 16 + j * 1024, 0, 16);
          ok &= (pp[j].y == tag) & (pp[j].w == tag);
        }
        if (__all(ok)) break;
        __builtin_amdgcn_s_sleep(1);
      }
      float y0 = 0.f, y1 = 0.f;
#pragma unroll
      for (int j = 0; j < 7; ++j) { y0 += __uint_as_float(pp[j].x); y1 += __uint_as_float(pp[j].z); }
      y0 += dppf<ROR(4)>(y0); y1 += dppf<ROR(4)>(y1);
      y0 += dppf<ROR(8)>(y0); y1 += dppf<ROR(8)>(y1);
      y0 = sum32(sum16(y0)); y1 = sum32(sum16(y1));
      STAMP(7);
      u32 rb = key & 0xffff0000u;
      rb ^= ((int)rb < 0) ? 0x80000000u : 0xffff0000u;
      const float sg = rbf(__builtin_amdgcn_rcpf(1.f + __builtin_amdgcn_exp2f(__uint_as_float(rb) * -1.44269504f)));
      const u32 o = lds_ld<u32>(sm, YO_BASE + row * 8 + (row >> 4) * 8 + (lane & 1) * 4);
      asm volatile("s_waitcnt vmcnt(0)" ::: "memory");
      if (own && lane < 4)
        *(u32*)(out + row * 512 + 4 * grp + 2 * (lane & 1)) = pkbf(bflo(o) + rbf(rbf(y0) * sg), bfhi(o) + rbf(rbf(y1) * sg));
    }
  }
  STAMP(8);
  STAMP_END(15);
}

extern "C" void kernel_launch(void* const* d_in, const int* in_sizes, int n_in, void* d_out, int out_size, void* d_ws,
                              size_t ws_size, hipStream_t stream) {
  fused_kernel<<<256, 512, 0, stream>>>(
      (const u16*)d_in[0], (const u16*)d_in[1], (const u16*)d_in[2], (const u16*)d_in[3], (const u16*)d_in[4],
      (const u16*)d_in[5], (const u16*)d_in[6], (const u16*)d_in[7], (const u16*)d_in[8], (const u16*)d_in[10],
      (const u16*)d_in[11], (const u16*)d_in[12], (const u16*)d_in[13], (u16*)d_out, (char*)d_ws);
}
```

```cpp
#include <hip/hip_runtime.h>
#include <stdint.h>

typedef unsigned short u16;
typedef unsigned int u32;
typedef unsigned long long u64;
typedef u32 u32x2 __attribute__((ext_vector_type(2)));
typedef u32 u32x4 __attribute__((ext_vector_type(4)));
typedef float f32x4 __attribute__((ext_vector_type(4)));
typedef float f32x2 __attribute__((ext_vector_type(2)));
typedef _Float16 h2 __attribute__((ext_vector_type(2)));
typedef _Float16 h8 __attribute__((ext_vector_type(8)));
typedef __bf16 b2 __attribute__((ext_vector_type(2)));
typedef __bf16 b8 __attribute__((ext_vector_type(8)));
typedef short s4 __attribute__((ext_vector_type(4)));
typedef u16 us2 __attribute__((ext_vector_type(2)));
#define LAS __attribute__((address_space(3)))
#define DEV __device__ __forceinline__

#define WS_CTRL 0
#define WS_STATS 4096
#define WS_KEYS 32768
#define WS_ACKS 65536
#define WS_PART 131072
#define WS_BYTES (1u << 20)
#define NPROD 112
#define NKEY 206
#define SPIN_CAP 100000

#define TAB_RS 288
#define TAB_W (16 * TAB_RS)
#define YO_BASE 0
#define U_BASE 36864
#define UCH 4576
#define FL_BASE (U_BASE + 4 * UCH)
#define KB_BASE (FL_BASE + 3072)
#define KL_CS 1216
#define Z2_OFF (4 * KL_CS + 16)
#define Z3_OFF (Z2_OFF + 512)
#define KCH (Z3_OFF + 320)
#define ST_BASE (KB_BASE + 4 * KCH)
#define ST_W 1152
#define INV_OFF (ST_BASE + 8 * ST_W + 2048)
#define W_BASE (INV_OFF + 64)
#define W3_OFF 32
#define W5_OFF (W3_OFF + 3200)
#define W7_OFF (W5_OFF + 3104)
#define W9_OFF (W7_OFF + 2128)
#define WCH (W9_OFF + 2048)
#define LDS_BYTES (W_BASE + 4 * WCH)
#define AX_GU 8192
#define AX_WD (AX_GU + 32768)
#define AX_KS (FL_BASE + 4096)

#define P_U1 2
#define P_W0A 3
#define P_U2 4
#define P_W1A 5
#define P_U3 6
#define P_ALL 7
#define CODE_OFF 4096
#define CODE_STEP 3968

#define STAMP(i)
#define STAMP_DECL
#define STAMP_END(i)
#define FST(i)
#define XST(s) s
#define SB() __builtin_amdgcn_sched_barrier(0)
#define PIN(v) asm volatile("" : "+v"(v))
#define CAS __attribute__((address_space(4)))
#define GAS __attribute__((address_space(1)))

DEV float bflo(u32 v) { return __uint_as_float(v << 16); }
DEV float bfhi(u32 v) { return __uint_as_float(v & 0xffff0000u); }
DEV u32 pkbf(float a, float b) { f32x2 f = {a, b}; return __builtin_bit_cast(u32, __builtin_convertvector(f, b2)); }
DEV float rbf(float a) { return bflo(pkbf(a, a)); }
DEV u32 pkh(float a, float b) { h2 h = {(_Float16)a, (_Float16)b}; return __builtin_bit_cast(u32, h); }
DEV u32 hadd(u32 a, u32 b) { return __builtin_bit_cast(u32, __builtin_bit_cast(h2, a) + __builtin_bit_cast(h2, b)); }
DEV f32x4 mma_h(u32x4 a, u32x4 b, f32x4 c) {
  return __builtin_amdgcn_mfma_f32_16x16x32_f16(__builtin_bit_cast(h8, a), __builtin_bit_cast(h8, b), c, 0, 0, 0);
}
DEV f32x4 mma_b(u32x4 a, u32x4 b, f32x4 c) {
  return __builtin_amdgcn_mfma_f32_16x16x32_bf16(__builtin_bit_cast(b8, a), __builtin_bit_cast(b8, b), c, 0, 0, 0);
}
template <int C> DEV u32 dppu(u32 v) { return __builtin_amdgcn_update_dpp(0, v, C, 0xf, 0xf, true); }
template <int C> DEV float dppf(float v) { return __uint_as_float(dppu<C>(__float_as_uint(v))); }
#define ROR(n) (0x120 + (n))
DEV float sum16(float v) { u32 u = __float_as_uint(v); auto r = __builtin_amdgcn_permlane16_swap(u, u, false, false); return __uint_as_float(r[0]) + __uint_as_float(r[1]); }
DEV u32 max16(u32 u) { auto r = __builtin_amdgcn_permlane16_swap(u, u, false, false); return max(r[0], r[1]); }
DEV u32 rowmax(u32 v) {
  v = max(v, dppu<ROR(1)>(v)); v = max(v, dppu<ROR(2)>(v)); v = max(v, dppu<ROR(4)>(v)); v = max(v, dppu<ROR(8)>(v));
  return v;
}
DEV u32 wavemax(u32 v) {
  v = rowmax(v);
  u32 a = __builtin_amdgcn_readlane(v, 0), b = __builtin_amdgcn_readlane(v, 16), c = __builtin_amdgcn_readlane(v, 32), d = __builtin_amdgcn_readlane(v, 48);
  return max(max(a, b), max(c, d));
}
DEV float trace4(f32x4 d) { return ((d.x + dppf<0x55>(d.y)) + dppf<0xaa>(d.z)) + dppf<0xff>(d.w); }
DEV void bar() { asm volatile("s_waitcnt lgkmcnt(0)\n\ts_barrier" ::: "memory"); }
DEV void bar0() { asm volatile("s_barrier" ::: "memory"); }

DEV float fma1(float a, float b, float c) { float r; asm("v_fma_f32 %0, %1, %2, %3" : "=v"(r) : "v"(a), "v"(b), "v"(c)); return r; }
#define CM_(t, o, a, b) "v_pk_mul_" t " " o ", " a ", " b " op_sel:[1,0] op_sel_hi:[1,1]\n\t"
#define CF_(t, o, a, b) "v_pk_fma_" t " " o ", " b ", " a ", " o " op_sel:[1,0,0] op_sel_hi:[0,0,1] neg_hi:[1,0,0]\n\t"
#define CM(o, a, b) CM_("f32", o, a, b) CF_("f32", o, a, b)
#define CM2(o, a, b, o2, a2, b2) CM_("f32", o, a, b) CM_("f32", o2, a2, b2) CF_("f32", o, a, b) CF_("f32", o2, a2, b2)
#define HM(o, a, b) CM_("f16", o, a, b) CF_("f16", o, a, b)
#define WT(v, row) "ds_write_addtid_b32 " v " offset:" #row "*288\n\t"
DEV f32x2 seed(f32x2 lg) {
  float e = __builtin_amdgcn_exp2f(lg.x), f = __builtin_amdgcn_fractf(lg.y);
  f32x2 o = {__builtin_amdgcn_sinf(f), __builtin_amdgcn_cosf(f)};
  return o * e;
}
DEV u32 toh(f32x2 a) { return pkh(a.x, a.y); }

template <typename T> DEV T lds_ld(LAS char* s, u32 off) { return *(LAS T*)(s + off); }
template <typename T> DEV T lds_vld(LAS char* s, u32 off) { return *(volatile LAS T*)(s + off); }
template <typename T> DEV void lds_st(LAS char* s, u32 off, T v) { *(LAS T*)(s + off) = v; }
template <typename T> DEV void lds_vst(LAS char* s, u32 off, T v) { *(volatile LAS T*)(s + off) = v; }
DEV u32x2 lds_tr(LAS char* s, u32 off) { return __builtin_bit_cast(u32x2, __builtin_amdgcn_ds_read_tr16_b64_v4i16((LAS s4*)(s + off))); }
DEV u32x4 lds_tr2(LAS char* s, u32 o0, u32 o1) { u32x2 a = lds_tr(s, o0), b = lds_tr(s, o1); u32x4 r = {a.x, a.y, b.x, b.y}; return r; }
template <typename T> DEV T ntld(const void* p) { return __builtin_nontemporal_load((const T*)p); }
#define WAITF(OFF, addr, cur0, C, n) do {                                                                    \
  u32 cnt_, cur_ = (cur0);                                                                                     \
  asm volatile(                                                                                                \
      "v_cmp_gt_u32 vcc, %3, %0\n\ts_cbranch_vccz 2f\n\ts_mov_b32 %1, %4\n"                                    \
      "1:\n\tds_read_b32 %0, %2 offset:%5\n\ts_sub_u32 %1, %1, 1\n\ts_waitcnt lgkmcnt(0)\n\tv_cmp_gt_u32 vcc, %3, %0\n\t" \
      "s_cbranch_scc1 2f\n\ts_cbranch_vccnz 1b\n2:"                                                            \
      : "+v"(cur_), "=&s"(cnt_) : "v"(addr), C(n), "n"(SPIN_CAP * 16), "n"(OFF) : "vcc", "scc", "memory");     \
} while (0)

__global__ __launch_bounds__(512, 2) void fused_kernel(
    const u16* __restrict__ x, const u16* __restrict__ pAre, const u16* __restrict__ pAim, const u16* __restrict__ pBre,
    const u16* __restrict__ pBim, const u16* __restrict__ pCre, const u16* __restrict__ pCim, const u16* __restrict__ pLdt,
    const u16* __restrict__ pDs, const u16* __restrict__ pWr, const u16* __restrict__ pWg, const u16* __restrict__ pWu,
    const u16* __restrict__ pWd, u16* __restrict__ out, char* ws) {
  __shared__ __attribute__((aligned(16))) char smem[LDS_BYTES];
  LAS char* sm = (LAS char*)smem;
  const char* pc0 = (const char*)__builtin_amdgcn_s_getpc();
  const int tid = threadIdx.x, lane = tid & 63;
  const int w = __builtin_amdgcn_readfirstlane(tid >> 6);
  const int c = lane & 15, g = lane >> 4, q = (lane >> 2) & 3, p = lane & 3;
  const int b = blockIdx.x, xs = b & 7, idx = b >> 3;
  const f32x4 z4 = {0.f, 0.f, 0.f, 0.f};
  const u32x4 zz = {0u, 0u, 0u, 0u};
  STAMP_DECL;
  STAMP(0);
  asm volatile("" :: "s"(x), "s"(pAre), "s"(pAim), "s"(pBre), "s"(pBim), "s"(pCre), "s"(pCim), "s"(pLdt), "s"(pDs), "s"(pWr), "s"(pWg),
               "s"(pWu), "s"(pWd), "s"(out), "s"(ws));
  const __amdgpu_buffer_rsrc_t wr = __builtin_amdgcn_make_buffer_rsrc(ws, 0, WS_BYTES, 0x00020000);
  const int dbl = (3 - (int)((size_t)out >> 7)) & 7;

  if (idx < 16 && xs != dbl) {
    const int fi = (xs - (xs > dbl)) * 16 + idx;
    const u32x4 ones = {0x3f803f80u, 0x3f803f80u, 0x3f803f80u, 0x3f803f80u};
    const int wq = w & 3, un = 5 * fi + w;
    const bool prod = w < 5 && un < 512;
    u32x4 xv[4], wv[4];
    u32 pf = 0;
    if (prod) {
      const u16* xr = x + (4 * un + q) * 512 + 32 * g + 8 * p;
#pragma unroll
      for (int j = 0; j < 4; ++j) xv[j] = ntld<u32x4>(xr + 128 * j);
#pragma unroll
      for (int j = 0; j < 4; ++j) wv[j] = *(const u32x4*)(pWr + 32 * g + 8 * p + 128 * j);
    } else if (w == 7) {
      pf = __builtin_nontemporal_load((const GAS u32*)(pc0 + lane * 128));
    }
    if (w == 7) lds_st<u32>(sm, AX_KS + lane * 4, 0u);
    bar();
    u32 tag = *(const CAS u32*)ws + 1u;
    if (prod) {
      f32x4 d0 = z4, d1 = z4, d2 = z4, d3 = z4;
#pragma unroll
      for (int j = 0; j < 4; ++j) { d2 = mma_b(xv[j], xv[j], d2); d0 = mma_b(ones, xv[j], d0); }
#pragma unroll
      for (int j = 0; j < 4; ++j) { d1 = mma_b(wv[j], xv[j], d1); d3 = mma_b(ones, wv[j], d3); }
      STAMP(1);
      const float mean = trace4(d0) * (1.f / 512.f);
      const float var = fmaxf(__builtin_fmaf(-mean, mean, trace4(d2) * (1.f / 512.f)), 0.f);
      const float sd = __builtin_amdgcn_sqrtf(rbf(rbf(var) + 1e-5f));
      const u32 ms = pkbf(mean, sd);
      const bool lead = lane == 20 * g;
      u32x2 gs = {ms, tag};
      if (lead) __builtin_amdgcn_raw_buffer_store_b64(gs, wr, WS_STATS + un * 32 + g * 8, 0, 16);
      const float r = __builtin_fmaf(-bflo(ms), trace4(d3), trace4(d1)) * __builtin_amdgcn_rcpf(bfhi(ms));
      u32 rb = pkbf(r, r) & 0xffff0000u;
      rb ^= ((int)rb < 0) ? 0xffff0000u : 0x80000000u;
      u32x2 km = {rb | (u32)(2048 - (4 * un + g)), ms};
      if (lead) lds_vst<u32x2>(sm, AX_KS + w * 32 + g * 8, km);
      STAMP(2);
      if (w == 0) {
        const bool need = (lane & 31) < min(20, 2048 - 20 * fi);
#pragma unroll 1
        for (int spin = 0; spin < SPIN_CAP * 16; ++spin) {
          km = lds_vld<u32x2>(sm, AX_KS + (lane & 31) * 8);
          if (!__any(need && km.x == 0u)) break;
        }
        const u32 k1 = max16(rowmax(km.x));
        const u32 k2 = max16(rowmax(km.x == k1 ? 0u : km.x));
        u32x4 gk = {km.x, tag, km.y, tag};
        if (lane < 32 && km.x >= k2) __builtin_amdgcn_raw_buffer_store_b128(gk, wr, WS_KEYS + fi * 32 + (km.x == k1 ? 0 : 16), 0, 16);
        STAMP(7);
      }
    } else if (w >= 5) {
      const u16* src = w == 7 ? pWd + 16 * fi * 512 + 8 * lane : (w == 6 ? pWu : pWg) + (lane >> 1) * 1792 + 16 * fi + 8 * (lane & 1);
      const int sstep = w == 7 ? 512 : 32 * 1792, dstep = w == 7 ? 1056 : 1024;
      LAS char* dst = sm + AX_GU + (w - 5) * 16384;
#pragma unroll
      for (int i = 0; i < 16; ++i) __builtin_amdgcn_global_load_lds((const GAS void*)(src + i * sstep), dst + i * dstep, 16, 0, 2);
      STAMP(1);
      asm volatile("s_waitcnt vmcnt(0)" ::: "memory");
      PIN(pf);
      STAMP(2);
    }
    asm volatile("" : "+s"(tag));
    bar0();
    u32x4 kk[4];
    const u32 ko = WS_KEYS + lane * 16;
#pragma unroll
    for (int j = 0; j < 4; ++j) kk[j] = __builtin_amdgcn_raw_buffer_load_b128(wr, j == 3 && lane >= NKEY - 192 ? ~15u : ko + j * 1024, 0, 16);
    u32x4 fa[4], fb[2];
    f32x4 ds = z4;
#pragma unroll
    for (int j = 0; j < 4; ++j) {
      const u32 o = AX_GU + w * 4096 + j * 1024 + (8 * g + q) * 32 + 8 * p;
      fa[j] = lds_tr2(sm, o, o + 128);
      ds = mma_b(fa[j], ones, ds);
    }
#pragma unroll
    for (int j = 0; j < 2; ++j) {
      const u32 o = AX_WD + (4 * g + q) * 1056 + 128 * w + j * 64 + 8 * p;
      fb[j] = lds_tr2(sm, o, o + 32);
    }
    PIN(ds); PIN(fb[0]); PIN(fb[1]);
    STAMP(3);
#pragma unroll 1
    for (int spin = 0; spin < SPIN_CAP; ++spin) {
      bool ok = ((kk[3].y == tag) & (kk[3].w == tag)) | (lane >= NKEY - 192);
#pragma unroll
      for (int j = 0; j < 3; ++j) ok &= (kk[j].y == tag) & (kk[j].w == tag);
      if (__all(ok)) break;
      asm volatile("" ::: "memory");
#pragma unroll
      for (int j = 0; j < 4; ++j) kk[j] = __builtin_amdgcn_raw_buffer_load_b128(wr, j == 3 && lane >= NKEY - 192 ? ~15u : ko + j * 1024, 0, 16);
    }
    STAMP(4);
    const u32 K1 = wavemax(max(max(kk[0].x, kk[1].x), max(kk[2].x, kk[3].x)));
    u32 m2 = 0;
#pragma unroll
    for (int j = 0; j < 4; ++j) m2 = max(m2, kk[j].x == K1 ? 0u : kk[j].x);
    const u32 K2 = wavemax(m2);
    const u32 key = (q & 1) ? K2 : K1;
    const u32 row = (2048 - (key & 0xfff)) & 2047;
    const u16* xr = x + row * 512;
#pragma unroll
    for (int j = 0; j < 4; ++j) xv[j] = *(const u32x4*)(xr + 128 * wq + 8 * g + 32 * j);
    const u32x2 xres = *(const u32x2*)(xr + 64 * w + 16 * p + 4 * g);
    SB();
    u32 s1 = 0, s2 = 0;
#pragma unroll
    for (int j = 0; j < 4; ++j) { s1 = kk[j].x == K1 ? kk[j].z : s1; s2 = kk[j].x == K2 ? kk[j].z : s2; }
    s1 = wavemax(s1); s2 = wavemax(s2);
    const u32 ms = (q & 1) ? s2 : s1;
    u32 rb = key & 0xffff0000u;
    rb ^= ((int)rb < 0) ? 0x80000000u : 0xffff0000u;
    float sg = rbf(__builtin_amdgcn_rcpf(1.f + __builtin_amdgcn_exp2f(__uint_as_float(rb) * -1.44269504f)));
    float rs = __builtin_amdgcn_rcpf(bfhi(ms));
    f32x4 d = ds * -bflo(ms);
    PIN(sg); PIN(rs); PIN(d);
    SB();
#pragma unroll
    for (int j = 0; j < 4; ++j) d = mma_b(fa[j], xv[j], d);
    {
      const float dg = p == 0 ? d.x : p == 1 ? d.y : p == 2 ? d.z : d.w;
      if (c < 8) lds_st<float>(sm, (((w >> 2) * 2 + q) * 16 + 4 * g + p) * 16 + 4 * wq, dg);
    }
    f32x4 ci = {bflo(xres.x), bfhi(xres.x), bflo(xres.y), bfhi(xres.y)};
    if (fi != 0) ci = z4;
    bar();
    STAMP(5);
    {
      const f32x4 s = lds_ld<f32x4>(sm, (q * 16 + 4 * g + p) * 16);
      const float v = rbf(((s.x + s.y) + (s.z + s.w)) * rs);
      const float o = dppf<ROR(8)>(v);
      const float ga = (c & 8) ? o : v, up = (c & 8) ? v : o;
      const float sl = ga * __builtin_amdgcn_rcpf(1.f + __builtin_amdgcn_exp2f(ga * -1.44269504f));
      const float hv = rbf(sl) * (up * sg);
      const u32 hp = pkbf(hv, dppf<0xb1>(hv));
      const u32 h0 = dppu<0x00>(hp), h1 = dppu<0xaa>(hp);
      const u32x4 b0 = {p == 0 ? h0 : 0u, p == 0 ? h1 : 0u, p == 1 ? h0 : 0u, p == 1 ? h1 : 0u};
      const u32x4 b1 = {p == 2 ? h0 : 0u, p == 2 ? h1 : 0u, p == 3 ? h0 : 0u, p == 3 ? h1 : 0u};
      const f32x4 y = mma_b(fb[1], b1, mma_b(fb[0], b0, ci));
      const u32 tw = tag ^ (row << 21);
      u32x4 gp = {pkbf(y.x, y.y), tw, pkbf(y.z, y.w), tw};
      if (c < 8) __builtin_amdgcn_raw_buffer_store_b128(gp, wr, WS_PART + ((16 * w + 4 * p + g) * NPROD + fi) * 32 + q * 16, 0, 16);
    }
    STAMP(6);
    STAMP_END(15);
    return;
  }

  const int grp = xs * 16 + (idx & 15);
  const int ch = w & 3, hb = w >> 2;
  const int chan = 4 * grp + ch;
  const u32 smask = xs != dbl ? 0xffu : idx < 16 ? 0x55u : 0xaau;
  u32 vAre, vAim, vBre, vBim, vCre, vCim, sld, sds, tag, pf;
  asm volatile("global_load_dword %0, %9, %10 nt\n\tglobal_load_dword %1, %9, %11 nt\n\tglobal_load_dword %2, %9, %12 nt\n\t"
               "global_load_dword %3, %9, %13 nt\n\tglobal_load_dword %4, %9, %14 nt\n\tglobal_load_dword %5, %9, %15 nt\n\t"
               "s_load_dword %6, %16, %19\n\ts_load_dword %7, %17, %19\n\ts_load_dword %8, %18, 0"
               : "=&v"(vAre), "=&v"(vAim), "=&v"(vBre), "=&v"(vBim), "=&v"(vCre), "=&v"(vCim), "=&s"(sld), "=&s"(sds), "=&s"(tag)
               : "v"(chan * 256 + lane * 4), "s"(pAre), "s"(pAim), "s"(pBre), "s"(pBim), "s"(pCre), "s"(pCim), "s"(pLdt), "s"(pDs), "s"(ws),
                 "s"((chan & ~1) * 2) : "memory");
  u32x2 xc[8];
  const u32 go = (128 * ch + 2 * lane) * 1024 + grp * 8;
  {
    const char* pb = w < 2 ? pc0 + CODE_OFF + w * CODE_STEP : (const char*)(x + ((idx & 15) * 64 + (3 - w) * 1024) * 512 + xs * 64);
    asm volatile("s_cmp_eq_u32 %5, 0\n\ts_cbranch_scc1 1f\n\t"
                 "global_load_dwordx2 %0, %6, %7\n\tglobal_load_dwordx2 %1, %6, %7 offset:1024\n\t"
                 "global_load_dwordx2 %2, %6, %8\n\tglobal_load_dwordx2 %3, %6, %8 offset:1024\n\ts_branch 2f\n"
                 "1:\n\tglobal_load_dword %4, %9, %10 nt\n2:"
                 : "=&v"(xc[0]), "=&v"(xc[1]), "=&v"(xc[2]), "=&v"(xc[3]), "=&v"(pf)
                 : "s"(hb), "v"(go), "s"(x), "s"(x + 262144), "v"(lane << (w < 2 ? 7 : 10)), "s"(pb) : "scc", "memory");
  }
#pragma unroll
  for (int i = 0; i < 3; ++i) lds_st<u32x4>(sm, FL_BASE + i * 8192 + tid * 16, zz);
  {
    const int k = tid >> 5, l = tid & 31;
    u32 o = U_BASE + k * UCH + min(l, 29) * 16;
    if (k == 4) { const int a = l >> 1 & 3; o = W_BASE + (l >> 3) * WCH + (a == 0 ? W3_OFF : a == 1 ? W5_OFF : a == 2 ? W7_OFF : W9_OFF) - 32 + (l & 1) * 16; }
    if (k > 4) o = ST_BASE - 16 - (tid - 160) * 16;
    if (tid < 256) lds_st<u32x4>(sm, o, zz);
  }
  const u32 tb = w * TAB_W;
  const u32 kb = KB_BASE + ch * KCH;
  u32 ta = tb + g * 16 + c * TAB_RS;
  u32 t1a = tb + g * 16 + (u32)((0xB766654443222100ull >> (c * 4)) & 15) * TAB_RS;
  u32 sa = ST_BASE + w * ST_W + (g >> 1) * 640 + 16 * c + 8 * (g & 1);
  u32 zs = ST_BASE + 8 * ST_W + ch * 512 + 8 * lane;
  u32 fl = FL_BASE + p * 256;
  u32 flw = FL_BASE + ch * 256 + lane * 4;
  u32 k0, k1, k2, k3, k4, k5, k6, k7, k8, k9, k10, k11, k12;
  if (hb == 0) {
    k0 = kb + (g < 2 ? Z2_OFF + 120 + 128 * g + 8 * c : Z3_OFF + 60 + 128 * (g - 2) + 4 * c);
    k1 = k0 + (g < 2 ? 4 : 64);
    k2 = ST_BASE + ch * ST_W + (lane & 1) * 640 + 16 * (lane >> 1);
    k3 = kb + 32 + 16 * lane;
    const int e = min(lane >> 1, 6), cp = lane & 1;
    const int dd = e < 4 ? 256 + (e & 1) : e == 4 ? 277 : 278;
    const bool odd = e == 2 || e == 3 || e == 6;
    k4 = kb + (2 * cp + (odd ? 1 : 0)) * KL_CS + 4 * dd + (odd ? 36 : cp ? 40 : 32) - 4 * cp;
    k5 = ((e == 1 || e == 5) ? 0x0c0c0302u : e == 4 ? 0x03020c0cu : 0x03020302u) | (e < 4 ? 0u : 0x04040404u);
    k6 = kb + 8 * p + ((-q) & 3) * KL_CS + 56 - 16 * g;
    k7 = kb + Z2_OFF + 8 * c + 16 * g;
    k8 = kb + Z3_OFF + 4 * c + 16 * g;
    k9 = U_BASE + ch * UCH + 32 * c + 16 * g;
    k10 = W_BASE + ch * WCH + (q & 1 ? W5_OFF : W3_OFF) + 2 * (384 - 276 + 4) - 8 * (8 * g + 4 + q) + 8 * p;
    k11 = W_BASE + ch * WCH + (q & 1 ? W9_OFF : W7_OFF) + 2 * (1024 - 792 + 4) - 16 * (8 * g + 4 + q) + 8 * p;
    k12 = YO_BASE + ch * 8192 + 64 * c + 16 * (g ^ ((c >> 1) & 3));
  } else {
    k0 = U_BASE + ch * UCH + 464 + lane * 16;
    k1 = W_BASE + ch * WCH + lane * 16;
    k2 = YO_BASE + 512 * ch + ((lane * 8) ^ (lane & 48));
    k3 = WS_STATS + 1024 * ch + 16 * lane;
    k4 = k5 = k6 = k7 = k8 = k9 = k10 = k11 = k12 = 0;
  }
  const u32 usel = hb ? 0x03020c0cu : 0x01000c0cu;
  PIN(ta); PIN(t1a); PIN(sa); PIN(zs); PIN(fl); PIN(flw);
  PIN(k0); PIN(k1); PIN(k2); PIN(k3); PIN(k4); PIN(k5); PIN(k6); PIN(k7); PIN(k8); PIN(k9); PIN(k10); PIN(k11); PIN(k12);
  asm volatile("s_waitcnt lgkmcnt(0)\n\ts_add_u32 %2, %2, 1" : "+s"(sld), "+s"(sds), "+s"(tag) :: "scc", "memory");
  const float dt = rbf(__builtin_amdgcn_exp2f(__uint_as_float(ch & 1 ? sld & 0xffff0000u : sld << 16) * 1.44269504f));
  const f32x2 cst = {1.44269504f, 0.15915494f};
  f32x2 dc = cst * dt;
  float d1 = 1.f + __uint_as_float(ch & 1 ? sds & 0xffff0000u : sds << 16);
  const int be0 = (int)((__builtin_amdgcn_readfirstlane(__float_as_uint(d1)) >> 23) & 255) - 6;
  const u32 dtb = __builtin_amdgcn_readfirstlane(__float_as_uint(dt)) >> 16;
  if (tid >= 256 || lane) d1 = 0.f;
  PIN(dc); PIN(d1);
  asm volatile("s_mov_b32 m0, %6\n\ts_cmp_lg_u32 %7, 0\n\ts_waitcnt vmcnt(4)\n\ts_cbranch_scc1 1f\n\ts_waitcnt vmcnt(1)\n1:"
               : "+v"(vAre), "+v"(vAim), "+v"(vBre), "+v"(vBim), "+v"(vCre), "+v"(vCim) : "s"(tb), "s"(hb) : "scc", "memory");
  u32x4 st[4];
  u32x2 za;
  {
#define UP(v) __uint_as_float(__builtin_amdgcn_perm(v, v, usel))
#define RD(F, a) _Pragma("unroll") for (int k = 0; k < 4; ++k) F[k] = lds_ld<u32x4>(sm, a + k * 64)
#define LATE(i) asm volatile("s_cmp_eq_u32 %1, 0\n\ts_cbranch_scc1 1f\n\tglobal_load_dwordx2 %0, %2, %3 offset:%4\n1:"              \
                             : "=&v"(xc[i]) : "s"(hb), "v"(go), "s"(x + ((i) >> 1) * 262144), "n"(((i) & 1) * 1024) : "scc", "memory")
    const f32x2 av = {UP(vAre), UP(vAim)};
    const f32x2 lg = av * dc;
    STAMP(9);
    const f32x2 E16 = seed(lg * 16.f), E1 = seed(lg);
    f32x2 E32, E64, E128, E2, E4, E8;
    asm(CM2("%0", "%6", "%6", "%3", "%7", "%7") CM2("%1", "%0", "%0", "%4", "%3", "%3") CM2("%2", "%1", "%1", "%5", "%4", "%4")
        : "=&v"(E32), "=&v"(E64), "=&v"(E128), "=&v"(E2), "=&v"(E4), "=&v"(E8) : "v"(E16), "v"(E1));
    u32 q1, r1, r2, r3, r4, r5, r6, r7, r8, t0, t1;
    asm volatile("v_cvt_pk_f16_f32 %0, -%10, %11\n\tv_cvt_pk_f16_f32 %1, -%12, %13\n\tv_cvt_pk_f16_f32 %3, -%14, %15\n\t"
                 "v_cvt_pk_f16_f32 %7, -%16, %17\n\t"
                 WT("%18", 0) WT("%0", 1) HM("%2", "%0", "%1") WT("%1", 2) HM("%4", "%0", "%3") WT("%2", 3) HM("%5", "%1", "%3") WT("%3", 4)
                 HM("%6", "%2", "%3") WT("%4", 5) HM("%8", "%0", "%7") WT("%5", 6) HM("%9", "%1", "%7") WT("%6", 7) WT("%8", 9)
                 HM("%8", "%2", "%7") WT("%9", 10) HM("%9", "%3", "%7") WT("%8", 11) HM("%8", "%4", "%7") WT("%9", 12)
                 HM("%9", "%5", "%7") WT("%8", 13) HM("%8", "%6", "%7") WT("%9", 14) WT("%7", 8) WT("%8", 15)
                 : "=&v"(q1), "=&v"(r2), "=&v"(r3), "=&v"(r4), "=&v"(r5), "=&v"(r6), "=&v"(r7), "=&v"(r8), "=&v"(t0), "=&v"(t1)
                 : "v"(E16.x), "v"(E16.y), "v"(E32.x), "v"(E32.y), "v"(E64.x), "v"(E64.y), "v"(E128.x), "v"(E128.y), "v"(0x3c000000u) : "memory");
    u32x4 fq[4], fa[4];
    RD(fq, ta);
    LATE(4);
    us2 mb = __builtin_elementwise_max(__builtin_bit_cast(us2, vBre & 0x7fff7fffu), __builtin_bit_cast(us2, vBim & 0x7fff7fffu));
    us2 mc = __builtin_elementwise_max(__builtin_bit_cast(us2, vCre & 0x7fff7fffu), __builtin_bit_cast(us2, vCim & 0x7fff7fffu));
    mb += mc;
    const float gr = fmaxf(fmaxf(bflo(vAre), __uint_as_float(vAre)), 0.f) * (dc.x * 262400.f);
    const u32 lb = wavemax((u32)max(mb.x, mb.y) + (u32)fminf(gr, 65536.f));
    const int be = max((int)((lb + dtb) >> 7) - 250, be0);
    const int se = min(max(262 - be, 1), 253);
    const float sc = __uint_as_float((u32)se << 23);
    lds_st<u32>(sm, INV_OFF + ch * 4 + (lane ? 16 : 0), (u32)(254 - se) << 23);
    const f32x2 Bv = f32x2{UP(vBim), UP(vBre)} * dt, Cv = f32x2{UP(vCim), UP(vCre)} * sc;
    f32x2 P0, E256, R0, E512, E516;
    asm(CM2("%0", "%5", "%6", "%1", "%7", "%7") CM2("%2", "%0", "%1", "%3", "%1", "%1") CM("%4", "%3", "%8")
        : "=&v"(P0), "=&v"(E256), "=&v"(R0), "=&v"(E512), "=&v"(E516) : "v"(Cv), "v"(Bv), "v"(E128), "v"(E4));
    const u32 z1 = toh(E1), z2 = toh(E2), z4h = toh(E4), z8 = toh(E8);
    asm volatile(WT("%9", 0) HM("%0", "%9", "%10") HM("%1", "%9", "%11") WT("%0", 1) HM("%2", "%0", "%11") WT("%1", 2) HM("%3", "%9", "%12") WT("%2", 3)
                 HM("%4", "%0", "%12") WT("%3", 4) HM("%5", "%1", "%12") WT("%4", 5) HM("%6", "%2", "%12") WT("%5", 6) HM("%7", "%9", "%13") WT("%6", 7)
                 HM("%8", "%0", "%13") WT("%7", 8) HM("%7", "%1", "%13") WT("%8", 9) HM("%8", "%2", "%13") WT("%7", 10) HM("%7", "%3", "%13") WT("%8", 11)
                 HM("%8", "%4", "%13") WT("%7", 12) HM("%7", "%5", "%13") WT("%8", 13) HM("%8", "%6", "%13") WT("%7", 14) WT("%8", 15)
                 : "=&v"(r1), "=&v"(r2), "=&v"(r3), "=&v"(r4), "=&v"(r5), "=&v"(r6), "=&v"(r7), "=&v"(t0), "=&v"(t1)
                 : "v"(toh(P0)), "v"(z1), "v"(z2), "v"(z4h), "v"(z8) : "memory");
    RD(fa, ta);
    LATE(5);
    asm volatile(WT("%5", 0) HM("%0", "%5", "%6") HM("%1", "%5", "%7") WT("%0", 1) HM("%2", "%0", "%7") WT("%1", 2) HM("%3", "%5", "%8") WT("%2", 3)
                 HM("%4", "%0", "%8") WT("%3", 4) HM("%3", "%1", "%8") WT("%4", 5) HM("%4", "%2", "%8") WT("%3", 6)
                 "v_pk_mul_f16 %3, %5, %9 op_sel:[1,0] op_sel_hi:[1,1] neg_lo:[0,1]\n\t"
                 "v_pk_fma_f16 %3, %9, %5, %3 op_sel:[1,0,0] op_sel_hi:[0,0,1]\n\t" WT("%4", 7) WT("%3", 11)
                 : "=&v"(r1), "=&v"(r2), "=&v"(r3), "=&v"(t0), "=&v"(t1)
                 : "v"(toh(R0)), "v"(z2), "v"(z4h), "v"(z8), "v"(q1) : "memory");
    f32x4 a0 = {d1 * sc, 0.f, 0.f, 0.f};
#pragma unroll
    for (int k = 0; k < 4; ++k) a0 = mma_h(fa[k], fq[k], a0);
    RD(fa, t1a);
    LATE(6);
    f32x2 S0, S1, X0, X1, Y0, Y1;
    asm(CM2("%0", "%6", "%8", "%1", "%7", "%8") CM2("%2", "%0", "%8", "%3", "%1", "%8") CM2("%4", "%2", "%9", "%5", "%3", "%9")
        : "=&v"(S0), "=&v"(S1), "=&v"(X0), "=&v"(X1), "=&v"(Y0), "=&v"(Y1) : "v"(P0), "v"(R0), "v"(E516), "v"(E512));
    lds_st<u32x2>(sm, sa, u32x2{pkh(a0.x, a0.y), pkh(a0.z, a0.w)});
    f32x4 a1 = z4;
#pragma unroll
    for (int k = 0; k < 4; ++k) a1 = mma_h(fa[k], fq[k], a1);
    LATE(7);
    asm volatile(WT("%4", 0) HM("%0", "%4", "%10") WT("%5", 4) HM("%1", "%5", "%10") WT("%0", 1) HM("%2", "%4", "%11") WT("%1", 5) HM("%3", "%5", "%11") WT("%2", 2)
                 HM("%2", "%0", "%11") WT("%3", 6) HM("%3", "%1", "%11") WT("%2", 3) WT("%6", 8) HM("%2", "%6", "%11") WT("%3", 7) WT("%7", 10)
                 HM("%3", "%7", "%11") WT("%2", 9) WT("%8", 12) HM("%2", "%8", "%11") WT("%3", 11) WT("%9", 14) HM("%3", "%9", "%11") WT("%2", 13) WT("%3", 15)
                 : "=&v"(r1), "=&v"(r2), "=&v"(t0), "=&v"(t1)
                 : "v"(toh(S0)), "v"(toh(S1)), "v"(toh(X0)), "v"(toh(X1)), "v"(toh(Y0)), "v"(toh(Y1)), "v"(z4h), "v"(z8) : "memory");
    RD(fa, ta);
    lds_st<u32x2>(sm, sa + 256, u32x2{pkh(a1.x, a1.y), pkh(a1.z, a1.w)});
    STAMP(10);
    asm volatile("s_cmp_eq_u32 %4, 0\n\ts_cbranch_scc1 1f\n\t"
                 "global_load_dwordx4 %0, %5, %6 offset:-4096 sc1\n\tglobal_load_dwordx4 %1, %5, %6 sc1\n\t"
                 "global_load_dwordx4 %2, %5, %7 offset:-4096 sc1\n\tglobal_load_dwordx4 %3, %5, %7 sc1\n1:"
                 : "=&v"(st[0]), "=&v"(st[1]), "=&v"(st[2]), "=&v"(st[3]) : "s"(hb), "v"(k3), "s"(ws + 4096), "s"(ws + 12288) : "scc", "memory");
    f32x4 a2 = z4;
#pragma unroll
    for (int k = 0; k < 4; ++k) a2 = mma_h(fa[k], fq[k], a2);
    za = u32x2{pkh(a2.x, a2.y), pkh(a2.z, a2.w)};
    if (hb) lds_st<u32x2>(sm, zs, za);
  }
  STAMP(1);
  bar();
  STAMP(2);

  if (hb == 0) {
    {
      const u32x4 oa = lds_ld<u32x4>(sm, k2), ob = lds_ld<u32x4>(sm, k2 + 4 * ST_W);
      const u32x2 zb = lds_ld<u32x2>(sm, zs);
      const u32x4 o = {hadd(oa.x, ob.x), hadd(oa.y, ob.y), hadd(oa.z, ob.z), hadd(oa.w, ob.w)};
      lds_st<u32x4>(sm, k3, o);
      const u32 p2 = dppu<0x138>(o.z), p3 = dppu<0x138>(o.w);
      const u32 e0 = __builtin_amdgcn_alignbit(o.x, p3, 16), e1 = __builtin_amdgcn_alignbit(o.y, o.x, 16), e2 = __builtin_amdgcn_alignbit(o.z, o.y, 16),
                e3 = __builtin_amdgcn_alignbit(o.w, o.z, 16), em = __builtin_amdgcn_alignbit(p3, p2, 16);
      lds_st<u32x4>(sm, k3 + 3 * KL_CS, u32x4{e0, e1, e2, e3});
      lds_st<u32x4>(sm, k3 + 2 * KL_CS, u32x4{p3, o.x, o.y, o.z});
      lds_st<u32x4>(sm, k3 + KL_CS, u32x4{em, e0, e1, e2});
      lds_st<u32>(sm, k3 + 2 * KL_CS + 16, o.w);
      lds_st<u32>(sm, k3 + KL_CS + 16, e3);
      const u32 zx = hadd(za.x, zb.x), zy = hadd(za.y, zb.y);
      lds_st<u32>(sm, k0, zx); lds_st<u32>(sm, k1, zy);
      lds_st<u32>(sm, k4, __builtin_amdgcn_perm(__builtin_amdgcn_readlane(zy, 31), __builtin_amdgcn_readlane(o.w, 63), k5));
      asm volatile("" ::: "memory");
    }
    if (tid == 64) __builtin_amdgcn_raw_buffer_store_b32(tag, wr, WS_ACKS + b * 4, 0, 16);
    u32x4 Ka[18], Kz[6], Kt[4];
#define LDK(t) Ka[t] = lds_tr2(sm, k6 + 64 * (t) + 8, k6 + 64 * (t))
#define LDZ(i) do { u32x2 l_ = lds_ld<u32x2>(sm, k7 + 64 * (i)), h_ = lds_ld<u32x2>(sm, z2b + 64 * (i)); Kz[i] = u32x4{l_.x, l_.y, h_.x, h_.y}; } while (0)
#define LDT(i) Kt[i] = u32x4{lds_ld<u32>(sm, k8 + 64 * (i)), lds_ld<u32>(sm, k8 + 64 * (i) + 4), lds_ld<u32>(sm, k8 + 64 * (i) + 8), lds_ld<u32>(sm, k8 + 64 * (i) + 12)}
#define LDU(j, F) _Pragma("unroll") for (int e = 0; e < 8; ++e) F[e] = lds_ld<u32x4>(sm, k9 + 512 * (j) + 64 * e)
#define LDX(u) lds_tr2(sm, k10 + 256 * ((u) - 3) + 32, k10 + 256 * ((u) - 3))
#define LDY(u) lds_tr2(sm, k11 + 512 * ((u) - 4) + 64, k11 + 512 * ((u) - 4))
#define LDS17(T) lds_ld<u32x4>(sm, k9 + 480 + 2 * (256 * (T) - 1032))
#define OWN(F, A) _Pragma("unroll") for (int e = 0; e < 8; ++e) A = mma_h(Ka[7 - e], F[e], A)
#define FWD(F, A) _Pragma("unroll") for (int e = 0; e < 8; ++e) A = mma_h(Ka[15 - e], F[e], A)
#define PUT(T, A) do { lds_st<f32x4>(sm, k12 + 1024 * (T), A); lds_vst<u32>(sm, flw + 1024, (T) + 1); } while (0)
#define MIX(n, d) _Pragma("unroll") for (int i_ = 0; i_ < n; ++i_) {                                           \
    __builtin_amdgcn_sched_group_barrier(0x008, 1, 0); __builtin_amdgcn_sched_group_barrier(0x080, d, 0); }
    u32 z2b = k7 + 8;
    PIN(z2b);
    u32 f = lds_vld<u32>(sm, fl);
#pragma unroll
    for (int t = 7; t >= 0; --t) LDK(t);
    SB();
    STAMP(3);
    WAITF(0, fl, f, "n", 1);
    STAMP(4);
    u32x4 F0[8], F1[8], Xa, Xb, Yv, Sv;
    f32x4 c0 = z4, c1 = z4, c2, c3, c4, c5, c6, c7;
    LDU(0, F0);
    SB();
#pragma unroll
    for (int t = 15; t >= 8; --t) LDK(t);
    LDK(16);
    OWN(F0, c0);
    MIX(8, 2); SB();
    PUT(0, c0);
    LDU(1, F1); f = lds_vld<u32>(sm, fl);
    FWD(F0, c1); c2 = mma_h(Ka[16], F0[7], z4);
    MIX(9, 1); SB();
    FST(5);
    WAITF(0, fl, f, "n", P_U1);
    LDU(2, F0); f = lds_vld<u32>(sm, fl);
    OWN(F1, c1);
    MIX(8, 1); SB();
    PUT(1, c1);
#pragma unroll
    for (int i = 0; i < 6; ++i) LDZ(i);
    FWD(F1, c2); c3 = mma_h(Ka[16], F1[7], z4);
    MIX(9, 2); SB();
    FST(11);
    WAITF(0, fl, f, "n", P_W0A);
    Xa = LDX(3); Xb = LDX(4); LDU(3, F1); f = lds_vld<u32>(sm, fl);
    OWN(F0, c2); c2 = mma_h(Xa, Kz[1], c2); c2 = mma_h(Xb, Kz[0], c2);
    MIX(10, 2); SB();
#define FAR(cn, cm) cn = mma_h(Xa, Kz[3], cn); cm = mma_h(Xa, Kz[5], cm); cn = mma_h(Xb, Kz[2], cn); cm = mma_h(Xb, Kz[4], cm)
    PUT(2, c2);
    LDK(17);
#pragma unroll
    for (int i = 0; i < 4; ++i) LDT(i);
    c4 = mma_h(Ka[16], F0[7], z4); FAR(c3, c4); FWD(F0, c3);
    MIX(13, 1); SB();
    FST(12);
    WAITF(0, fl, f, "n", P_U2);
    Xa = LDX(5); Xb = LDX(6); LDU(4, F0); f = lds_vld<u32>(sm, fl);
    OWN(F1, c3); c3 = mma_h(Xa, Kz[1], c3); c3 = mma_h(Xb, Kz[0], c3);
    MIX(10, 2); SB();
    PUT(3, c3);
    c5 = mma_h(Ka[16], F1[7], z4); FAR(c4, c5); FWD(F1, c4);
    SB();
    FST(13);
    WAITF(0, fl, f, "n", P_W1A);
    Xa = LDX(7); Xb = LDX(8); Yv = LDY(4); Sv = LDS17(4); LDU(5, F1); f = lds_vld<u32>(sm, fl);
#define FIN(A) A = mma_h(Xa, Kz[1], A); A = mma_h(Xb, Kz[0], A); A = mma_h(Yv, Kt[0], A); A = mma_h(Ka[17], Sv, A)
    OWN(F0, c4); FIN(c4);
    MIX(12, 2); SB();
    PUT(4, c4);
    c6 = mma_h(Ka[16], F0[7], z4); FAR(c5, c6); c5 = mma_h(Yv, Kt[1], c5); c6 = mma_h(Yv, Kt[2], c6); c7 = mma_h(Yv, Kt[3], z4); FWD(F0, c5);
    SB();
    FST(14);
    WAITF(0, fl, f, "n", P_U3);
    Xa = LDX(9); Xb = LDX(10); Yv = LDY(5); Sv = LDS17(5); LDU(6, F0); f = lds_vld<u32>(sm, fl);
    OWN(F1, c5); FIN(c5);
    MIX(12, 2); SB();
    PUT(5, c5);
    c7 = mma_h(Ka[16], F1[7], c7); FAR(c6, c7); c6 = mma_h(Yv, Kt[1], c6); c7 = mma_h(Yv, Kt[2], c7); FWD(F1, c6);
    SB();
    WAITF(0, fl, f, "n", P_ALL);
    Xa = LDX(11); Xb = LDX(12); Yv = LDY(6); Sv = LDS17(6); LDU(7, F1);
    OWN(F0, c6); FIN(c6);
    MIX(12, 2); SB();
    PUT(6, c6);
    c7 = mma_h(Xa, Kz[3], c7); c7 = mma_h(Xb, Kz[2], c7); c7 = mma_h(Yv, Kt[1], c7);
    SB();
    Xa = LDX(13); Xb = LDX(14); Yv = LDY(7); Sv = LDS17(7);
    FWD(F0, c7); OWN(F1, c7); FIN(c7);
    MIX(7, 1); SB();
    PUT(7, c7);
    STAMP(6);
    asm volatile("s_waitcnt vmcnt(0)" : "+v"(pf));
    if (w == 0) {
      u32x4 pp[4];
      u32 t0, t1;
      const u32 po = WS_PART + grp * (NPROD * 32) + lane * 16;
#pragma unroll 1
      for (int spin = 0; spin < SPIN_CAP; ++spin) {
#pragma unroll
        for (int j = 0; j < 4; ++j) pp[j] = __builtin_amdgcn_raw_buffer_load_b128(wr, j == 3 && lane >= 32 ? ~15u : po + j * 1024, 0, 16);
        asm volatile("" ::: "memory");
        t0 = __builtin_amdgcn_readlane(pp[0].y, 0); t1 = __builtin_amdgcn_readlane(pp[0].y, 1);
        const u32 tw = (lane & 1) ? t1 : t0;
        bool ok = ((pp[3].y == tw) & (pp[3].w == tw)) | (lane >= 32);
#pragma unroll
        for (int j = 0; j < 3; ++j) ok &= (pp[j].y == tw) & (pp[j].w == tw);
        if (__all(ok) && (((t0 ^ tag) | (t1 ^ tag)) & 0x1fffffu) == 0u) break;
      }
      STAMP(7);
      const u32 row = (((lane & 32) ? t1 : t0) ^ tag) >> 21;
      const u32 sl = p & 1 ? 0x3f800000u : 0x3f80u;
      const u32x4 sel = {p < 2 ? sl : 0u, p < 2 ? 0u : sl, p < 2 ? sl : 0u, p < 2 ? 0u : sl};
      const f32x4 d = mma_b(u32x4{pp[2].x, pp[2].z, pp[3].x, pp[3].z}, sel, mma_b(u32x4{pp[0].x, pp[0].z, pp[1].x, pp[1].z}, sel, z4));
      if (b == 255) {
#pragma unroll 1
        for (int spin = 0; spin < SPIN_CAP; ++spin) {
          const u32x4 a = __builtin_amdgcn_raw_buffer_load_b128(wr, WS_ACKS + lane * 16, 0, 16);
          asm volatile("" ::: "memory");
          const bool lo = lane < 32;
          const int s0 = (lane & 1) * 4;
          if (__all((a.x == tag || (lo && s0 != dbl)) && (a.y == tag || (lo && s0 + 1 != dbl)) && (a.z == tag || (lo && s0 + 2 != dbl)) && (a.w == tag || (lo && s0 + 3 != dbl)))) break;
        }
        if (lane == 0) __builtin_amdgcn_raw_buffer_store_b32(tag, wr, WS_CTRL, 0, 16);
      }
      const u32 aw = FL_BASE + 2048 + ((row >> 7) & 3) * 256, an = row >> 9;
#pragma unroll 1
      for (int spin = 0; spin < SPIN_CAP * 16; ++spin)
        if (__all(lds_vld<u32>(sm, aw) > an)) break;
      const float yv = lds_vld<float>(sm, YO_BASE + p * 8192 + ((row * 4) ^ ((row >> 1) & 48)));
      const float iv = lds_ld<float>(sm, INV_OFF + p * 4);
      auto r = __builtin_amdgcn_permlane32_swap(__float_as_uint(d.x + d.z), __float_as_uint(d.y + d.w), false, false);
      const float o = __builtin_fmaf(yv, iv, sum16(__uint_as_float(r[0]) + __uint_as_float(r[1])));
      if ((lane & 31) < 4 && ((smask >> (row >> 8)) & 1)) out[row * 512 + 4 * grp + p] = (u16)pkbf(o, o);
      STAMP(8);
    }
  } else {
    const f32x4 inv = lds_ld<f32x4>(sm, INV_OFF);
    float xf[4][8];
#define UNP(i) do {                                                                                             \
    _Pragma("unroll") for (int e = 0; e < 2; ++e) {                                                             \
      xf[i][4 * e] = bflo(xc[2 * (i) + e].x); xf[i][4 * e + 1] = bfhi(xc[2 * (i) + e].x);                       \
      xf[i][4 * e + 2] = bflo(xc[2 * (i) + e].y); xf[i][4 * e + 3] = bfhi(xc[2 * (i) + e].y);                   \
    }                                                                                                           \
    _Pragma("unroll") for (int e = 0; e < 8; ++e) PIN(xf[i][e]);                                                \
  } while (0)
#define FLAG(n) lds_vst<u32>(sm, flw, n)
#define POLL(i, j) do {                                                                                         \
    _Pragma("unroll 1") for (int spin = 0; spin < SPIN_CAP; ++spin) {                                           \
      bool ok_ = true;                                                                                          \
      _Pragma("unroll") for (int k = i; k < j; ++k) ok_ &= (st[k].y == tag) & (st[k].w == tag);                 \
      if (__builtin_expect(__all(ok_), 1)) break;                                                               \
      asm volatile("" ::: "memory");                                                                            \
      _Pragma("unroll") for (int k = i; k < 4; ++k) st[k] = __builtin_amdgcn_raw_buffer_load_b128(wr, k3 + 4096 * k, 0, 16); \
    }                                                                                                           \
  } while (0)
#define MX(h, o, a, r, n) "v_fma_mix" h "_f16 " o ", " a ", " r ", " n "\n\t"
#define UB(i, n) do {                                                                                           \
    const float r0_ = __builtin_amdgcn_rcpf(bfhi(st[i].x)), r1_ = __builtin_amdgcn_rcpf(bfhi(st[i].z));         \
    float n0_ = bflo(st[i].x) * r0_, n1_ = bflo(st[i].z) * r1_;                                                 \
    PIN(n0_);                                                                                                   \
    u32 u0_, u1_, u2_, u3_;                                                                                     \
    asm volatile(MX("lo", "%0", "%4", "%12", "-%14") MX("lo", "%1", "%5", "%12", "-%14") MX("lo", "%2", "%6", "%12", "-%14") MX("lo", "%3", "%7", "%12", "-%14") \
                 MX("hi", "%0", "%8", "%13", "-%15") MX("hi", "%1", "%9", "%13", "-%15") MX("hi", "%2", "%10", "%13", "-%15") MX("hi", "%3", "%11", "%13", "-%15") \
                 "ds_write_addtid_b32 %0 offset:%16\n\tds_write_addtid_b32 %1 offset:%17\n\t"                   \
                 "ds_write_addtid_b32 %2 offset:%18\n\tds_write_addtid_b32 %3 offset:%19"                       \
                 : "=&v"(u0_), "=&v"(u1_), "=&v"(u2_), "=&v"(u3_)                                               \
                 : "v"(xf[i][0]), "v"(xf[i][1]), "v"(xf[i][2]), "v"(xf[i][3]), "v"(xf[i][4]), "v"(xf[i][5]), "v"(xf[i][6]), "v"(xf[i][7]), \
                   "v"(r0_), "v"(r1_), "v"(n0_), "v"(n1_), "n"(1024 * (i)), "n"(UCH + 1024 * (i)), "n"(2 * UCH + 1024 * (i)), "n"(3 * UCH + 1024 * (i)) : "memory"); \
    FLAG(n);                                                                                                    \
  } while (0)
    u32 r[8], od[7];
    u32x4 w5;
#define WINA(it, need, n) do {                                                                                  \
    WAITF(0, fl, lds_vld<u32>(sm, fl), "n", need);                                                               \
    const u32x4 ra = lds_ld<u32x4>(sm, k0 + 1024 * (it)), rb = lds_ld<u32x4>(sm, k0 + 16 + 1024 * (it));        \
    r[0] = ra.x; r[1] = ra.y; r[2] = ra.z; r[3] = ra.w; r[4] = rb.x; r[5] = rb.y; r[6] = rb.z; r[7] = rb.w;     \
    _Pragma("unroll") for (int i = 0; i < 7; ++i) od[i] = __builtin_amdgcn_alignbit(r[i + 1], r[i], 16);        \
    u32x4 w3;                                                                                                   \
    _Pragma("unroll") for (int j = 2; j < 6; ++j) {                                                             \
      w3[j - 2] = hadd(hadd(od[j - 1], od[j]), r[j]);                                                           \
      w5[j - 2] = hadd(hadd(r[j - 1], r[j + 1]), w3[j - 2]);                                                    \
    }                                                                                                           \
    lds_st<u32x4>(sm, k1 + W3_OFF + 1024 * (it), w3);                                                           \
    lds_st<u32x4>(sm, k1 + W5_OFF + 1024 * (it), w5);                                                           \
    FLAG(n);                                                                                                    \
  } while (0)
#define WINB(it) do {                                                                                           \
    u32x4 w7, w9;                                                                                               \
    _Pragma("unroll") for (int j = 2; j < 6; ++j) {                                                             \
      w7[j - 2] = hadd(hadd(od[j - 2], od[j + 1]), w5[j - 2]);                                                  \
      w9[j - 2] = hadd(hadd(r[j - 2], r[j + 2]), w7[j - 2]);                                                    \
    }                                                                                                           \
    lds_st<u32x4>(sm, k1 + W7_OFF + 1024 * (it), w7);                                                           \
    lds_st<u32x4>(sm, k1 + W9_OFF + 1024 * (it), w9);                                                           \
  } while (0)
    const bool sto = (smask >> (ch >> 1)) & 1;
#define STO(i) do { if (sto) {                                                                                  \
    WAITF(1024, fl, lds_vld<u32>(sm, fl + 1024), "s", 2 * (i) + 1 + (ch >> 1));                                  \
    f32x2 y_[4];                                                                                                \
    _Pragma("unroll") for (int k = 0; k < 4; ++k) y_[k] = lds_ld<f32x2>(sm, k2 + 8192 * k + 2048 * (i));        \
    _Pragma("unroll") for (int k = 0; k < 4; ++k) { y_[k].x = fma1(y_[k].x, inv[k], xf[i][k]); y_[k].y = fma1(y_[k].y, inv[k], xf[i][4 + k]); } \
    const u32x2 o0_ = {pkbf(y_[0].x, y_[1].x), pkbf(y_[2].x, y_[3].x)}, o1_ = {pkbf(y_[0].y, y_[1].y), pkbf(y_[2].y, y_[3].y)}; \
    asm volatile(XST("global_store_dwordx2 %0, %1, %3\n\tglobal_store_dwordx2 %0, %2, %3 offset:1024")                \
                 :: "v"(go), "v"(o0_), "v"(o1_), "s"(out + (i) * 262144) : "memory");                           \
  } } while (0)
#define ACK(cnt, n) do { asm volatile("s_waitcnt vmcnt(" #cnt ")" ::: "memory"); lds_vst<u32>(sm, flw + 2048, n); } while (0)
    asm volatile("s_mov_b32 m0, %2\n\ts_waitcnt vmcnt(10)" : "+v"(xc[0]), "+v"(xc[1]) : "s"(U_BASE + 480 + 256 * ch));
    UNP(0);
    STAMP(3);
    asm volatile("s_waitcnt vmcnt(0)" : "+v"(st[0]), "+v"(st[1]), "+v"(st[2]), "+v"(st[3]), "+v"(xc[2]), "+v"(xc[3]), "+v"(xc[4]), "+v"(xc[5]), "+v"(xc[6]), "+v"(xc[7]));
    POLL(0, 1);
    UB(0, 1);
    STAMP(4);
    UNP(1);
    POLL(1, 2);
    UB(1, P_U1);
    WINA(0, 1, P_W0A);
    POLL(2, 4);
    STAMP(5);
    STO(0);
    STAMP(7);
    UNP(2); UB(2, P_U2);
    FST(8);
    WINB(0);
    WINA(1, P_U1, P_W1A);
    FST(12);
    STO(1);
    FST(13);
    UNP(3); UB(3, P_U3);
    FST(14);
    WINB(1);
    WINA(2, P_U2, P_ALL);
    STAMP(11);
    ACK(2, 1);
    STO(2);
    ACK(2, 2);
    STO(3);
    STAMP(6);
    ACK(2, 3);
    ACK(0, 4);
  }
  STAMP_END(15);
}

extern "C" void kernel_launch(void* const* d_in, const int* in_sizes, int n_in, void* d_out, int out_size, void* d_ws,
                              size_t ws_size, hipStream_t stream) {
  fused_kernel<<<256, 512, 0, stream>>>(
      (const u16*)d_in[0], (const u16*)d_in[1], (const u16*)d_in[2], (const u16*)d_in[3], (const u16*)d_in[4],
      (const u16*)d_in[5], (const u16*)d_in[6], (const u16*)d_in[7], (const u16*)d_in[8], (const u16*)d_in[10],
      (const u16*)d_in[11], (const u16*)d_in[12], (const u16*)d_in[13], (u16*)d_out, (char*)d_ws);
}
```

```cpp
#define SLATE 1
#include <hip/hip_runtime.h>
#include <stdint.h>

typedef unsigned short u16;
typedef unsigned int u32;
typedef unsigned long long u64;
typedef u32 u32x2 __attribute__((ext_vector_type(2)));
typedef u32 u32x4 __attribute__((ext_vector_type(4)));
typedef float f32x4 __attribute__((ext_vector_type(4)));
typedef float f32x16 __attribute__((ext_vector_type(16)));
typedef float f32x2 __attribute__((ext_vector_type(2)));
typedef _Float16 h2 __attribute__((ext_vector_type(2)));
typedef _Float16 h8 __attribute__((ext_vector_type(8)));
typedef __bf16 b2 __attribute__((ext_vector_type(2)));
typedef __bf16 b8 __attribute__((ext_vector_type(8)));
typedef short s4 __attribute__((ext_vector_type(4)));
typedef u16 us2 __attribute__((ext_vector_type(2)));
#define LAS __attribute__((address_space(3)))
#define DEV __device__ __forceinline__

#define WS_CTRL 0
#define WS_STATS (1u << 20)
#define SREP 1
#define SCS 16640
#define KREP 1
#define KCS 4352
#define WS_KEYS 8192
#define WS_ACKS 65536
#define WS_PART 131072
#define WS_EX (768u << 10)
#define WS_BYTES (2u << 20)
#define NPROD 56
#define NKEY 206
#define SPIN_CAP 100000

#define TAB_RS 288
#define TAB_W (16 * TAB_RS)
#define YO_BASE 0
#define U_BASE 36864
#define UCH 4576
#define FL_BASE (U_BASE + 4 * UCH)
#define KB_BASE (FL_BASE + 3072)
#define KL_CS 1216
#define Z2_OFF (4 * KL_CS + 16)
#define Z3_OFF (Z2_OFF + 512)
#define KCH (Z3_OFF + 320)
#define ST_BASE (KB_BASE + 4 * KCH)
#define ST_W 1152
#define INV_OFF (ST_BASE + 8 * ST_W + 2048)
#define W_BASE (INV_OFF + 64)
#define W3_OFF 32
#define W5_OFF (W3_OFF + 3200)
#define W7_OFF (W5_OFF + 3104)
#define W9_OFF (W7_OFF + 2128)
#define WCH (W9_OFF + 2048)
#define LDS_BYTES (W_BASE + 4 * WCH)
#define AX_GU 8192
#define AX_WD (AX_GU + 65536)
#define SPF 1
#define SWP 1
#define CPF 0
#define FPF 2
#define FPW "\n\ts_waitcnt vmcnt(0)"
#define W7P 1
#define LAY 1
#define EXP1(c) __builtin_expect(c, 1)
#define EXP2(c) __builtin_expect(c, 1)
#define POLLW 5
#define AX_KS (FL_BASE + 4096)
#define PFPOL ""
#define WAUX 0
#define XPOL ""
#define PPOL "nt"
#define DAUX 2
#define RLD(p) ntld<u32x4>(p)
#define AX_X 4096
#define AX_I 6400

#define P_ST 1
#define P_U0 2
#define P_U1 3
#define P_W0A 4
#define P_U2 5
#define P_W1A 6
#define P_U3 7
#define P_ALL 8
#define YCH 9216
#define CODE_MAX 16128

#define STAMP(i)
#define STAMP_DECL
#define STAMP_END(i)
#define FST(i)
#define XST(s) s
#define PK 2
#define PM 0
#define PH 1
#define CPOL ""
#define SPOL ""
#define PLATE 1
#define KC 1
#define ARGE 1
#define COLM 2
#define NODBL
#define HORD 0
#define PFT 5
#define PRIO(n) __builtin_amdgcn_s_setprio(n)
#define SB() __builtin_amdgcn_sched_barrier(0)
#define PIN(v) asm volatile("" : "+v"(v))
#define CAS __attribute__((address_space(4)))
#define GAS __attribute__((address_space(1)))

DEV float bflo(u32 v) { return __uint_as_float(v << 16); }
DEV float bfhi(u32 v) { return __uint_as_float(v & 0xffff0000u); }
DEV u32 pkbf(float a, float b) { f32x2 f = {a, b}; return __builtin_bit_cast(u32, __builtin_convertvector(f, b2)); }
DEV float rbf(float a) { return bflo(pkbf(a, a)); }
DEV u32 pkh(float a, float b) { h2 h = {(_Float16)a, (_Float16)b}; return __builtin_bit_cast(u32, h); }
DEV u32 hadd(u32 a, u32 b) { return __builtin_bit_cast(u32, __builtin_bit_cast(h2, a) + __builtin_bit_cast(h2, b)); }
DEV f32x4 mma_h(u32x4 a, u32x4 b, f32x4 c) {
  return __builtin_amdgcn_mfma_f32_16x16x32_f16(__builtin_bit_cast(h8, a), __builtin_bit_cast(h8, b), c, 0, 0, 0);
}
DEV f32x4 mma_b(u32x4 a, u32x4 b, f32x4 c) {
  return __builtin_amdgcn_mfma_f32_16x16x32_bf16(__builtin_bit_cast(b8, a), __builtin_bit_cast(b8, b), c, 0, 0, 0);
}
template <int C> DEV u32 dppu(u32 v) { return __builtin_amdgcn_update_dpp(0, v, C, 0xf, 0xf, true); }
template <int C> DEV float dppf(float v) { return __uint_as_float(dppu<C>(__float_as_uint(v))); }
#define ROR(n) (0x120 + (n))
DEV float sum16(float v) { u32 u = __float_as_uint(v); auto r = __builtin_amdgcn_permlane16_swap(u, u, false, false); return __uint_as_float(r[0]) + __uint_as_float(r[1]); }
DEV u32 max16(u32 u) { auto r = __builtin_amdgcn_permlane16_swap(u, u, false, false); return max(r[0], r[1]); }
DEV u32 rowmax(u32 v) {
  v = max(v, dppu<ROR(1)>(v)); v = max(v, dppu<ROR(2)>(v)); v = max(v, dppu<ROR(4)>(v)); v = max(v, dppu<ROR(8)>(v));
  return v;
}
DEV u32 wavemax(u32 v) {
  v = rowmax(v);
  u32 a = __builtin_amdgcn_readlane(v, 0), b = __builtin_amdgcn_readlane(v, 16), c = __builtin_amdgcn_readlane(v, 32), d = __builtin_amdgcn_readlane(v, 48);
  return max(max(a, b), max(c, d));
}
DEV float trace4(f32x4 d) { return ((d.x + dppf<0x55>(d.y)) + dppf<0xaa>(d.z)) + dppf<0xff>(d.w); }
DEV void bar() { asm volatile("s_waitcnt lgkmcnt(0)\n\ts_barrier" ::: "memory"); }
DEV void bar0() { asm volatile("s_barrier" ::: "memory"); }

DEV float fma1(float a, float b, float c) { float r; asm("v_fma_f32 %0, %1, %2, %3" : "=v"(r) : "v"(a), "v"(b), "v"(c)); return r; }
#define CM_(t, o, a, b) "v_pk_mul_" t " " o ", " a ", " b " op_sel:[1,0] op_sel_hi:[1,1]\n\t"
#define CF_(t, o, a, b) "v_pk_fma_" t " " o ", " b ", " a ", " o " op_sel:[1,0,0] op_sel_hi:[0,0,1] neg_hi:[1,0,0]\n\t"
#define CM(o, a, b) CM_("f32", o, a, b) CF_("f32", o, a, b)
#define CM2(o, a, b, o2, a2, b2) CM_("f32", o, a, b) CM_("f32", o2, a2, b2) CF_("f32", o, a, b) CF_("f32", o2, a2, b2)
#define HM(o, a, b) CM_("f16", o, a, b) CF_("f16", o, a, b)
#define WT(v, row) "ds_write_addtid_b32 " v " offset:" #row "*288\n\t"
DEV f32x2 seed(f32x2 lg) {
  float e = __builtin_amdgcn_exp2f(lg.x), f = __builtin_amdgcn_fractf(lg.y);
  f32x2 o = {__builtin_amdgcn_sinf(f), __builtin_amdgcn_cosf(f)};
  return o * e;
}
DEV u32 toh(f32x2 a) { return pkh(a.x, a.y); }

template <typename T> DEV T lds_ld(LAS char* s, u32 off) { return *(LAS T*)(s + off); }
template <typename T> DEV T lds_vld(LAS char* s, u32 off) { return *(volatile LAS T*)(s + off); }
template <typename T> DEV void lds_st(LAS char* s, u32 off, T v) { *(LAS T*)(s + off) = v; }
template <typename T> DEV void lds_vst(LAS char* s, u32 off, T v) { *(volatile LAS T*)(s + off) = v; }
DEV u32x2 lds_tr(LAS char* s, u32 off) { return __builtin_bit_cast(u32x2, __builtin_amdgcn_ds_read_tr16_b64_v4i16((LAS s4*)(s + off))); }
DEV u32x4 lds_tr2(LAS char* s, u32 o0, u32 o1) { u32x2 a = lds_tr(s, o0), b = lds_tr(s, o1); u32x4 r = {a.x, a.y, b.x, b.y}; return r; }
template <typename T> DEV T ntld(const void* p) { return __builtin_nontemporal_load((const T*)p); }
#define WAITF(OFF, addr, cur0, C, n) do {                                                                    \
  u32 cnt_, cur_ = (cur0);                                                                                     \
  asm volatile(                                                                                                \
      "v_cmp_gt_u32 vcc, %3, %0\n\ts_cbranch_vccz 2f\n\ts_mov_b32 %1, %4\n"                                    \
      "1:\n\tds_read_b32 %0, %2 offset:%5\n\ts_sub_u32 %1, %1, 1\n\ts_waitcnt lgkmcnt(0)\n\tv_cmp_gt_u32 vcc, %3, %0\n\t" \
      "s_cbranch_scc1 2f\n\ts_cbranch_vccnz 1b\n2:"                                                            \
      : "+v"(cur_), "=&s"(cnt_) : "v"(addr), C(n), "n"(SPIN_CAP * 16), "n"(OFF) : "vcc", "scc", "memory");     \
} while (0)

#define MFT
#define MF_CLOB "v29", "v30", "v31", "v38", "v39", "v40", "v41", "v42", "v43", "v44", "v45", "v46", "v47", "v48", "v49", "v50", "v51", "v52", "v53", "v54", "v55", "v56", "v57", "v58", "v59", "v60", "v61", "v62", "v63", "v64", "v65", "v66", "v67", "v68", "v69", "v70", "v71", "v72", "v73", "v74", "v75", "v76", "v77", "v78", "v79", "v80", "v81", "v82", "v83", "v84", "v85", "v86", "v87", "v88", "v89", "v90", "v91", "v92", "v93", "v94", "v95", "v96", "v97", "v98", "v99", "v100", "v101", "v102", "v103", "v104", "v105", "v106", "v107", "v108", "v109", "v110", "v111", "v112", "v113", "v114", "v115", "v116", "v117", "v118", "v119", "v120", "v121", "v122", "v123", "v124", "v125", "v126", "v127", "v128", "v129", "v130", "v131", "v132", "v133", "v134", "v135", "v136", "v137", "v138", "v139", "v140", "v141", "v142", "v143", "v144", "v145", "v146", "v147", "v148", "v149", "v150", "v151", "v152", "v153", "v154", "v155", "v156", "v157", "v158", "v159", "v160", "v161", "v162", "v163", "v164", "v165", "v166", "v167", "v168", "v169", "v170", "v171", "v172", "v173", "v174", "v175", "v176", "v177", "v178", "v179", "v180", "v181", "v182", "v183", "v184", "v185", "v186", "v187", "v188", "v189", "v190", "v191", "v192", "v193", "v194", "v195", "v196", "v197", "v198", "v199", "v200", "v201", "v202", "v203", "v204", "v205", "v206", "v207", "v208", "v209", "v210", "v211", "v212", "v213", "v214", "v215", "v216", "v217", "v218", "v219", "v220", "v221", "v222", "v223", "v224", "v225", "v226", "v227", "v228", "v229", "v230", "v231", "v232", "v233", "v234", "v235", "v236", "v237", "v238", "v239", "v240", "v241", "v242", "v243", "v244", "v245", "v246", "v247", "v248", "v249", "v250", "v251", "v252", "v253"
#define MF_ASM \
  "ds_read_b32 v236, %13\nds_read_b64_tr_b16 v[56:57], %6 offset:456\nds_read_b64_tr_b16 v[58:59], %6 offset:448\nds_read_b64_tr_b16 v[52:53], %6 offset:392\nds_read_b64_tr_b16 v[54:55], %6 offset:384\n" \
  "ds_read_b64_tr_b16 v[48:49], %6 offset:328\nds_read_b64_tr_b16 v[50:51], %6 offset:320\nds_read_b64_tr_b16 v[44:45], %6 offset:264\nds_read_b64_tr_b16 v[46:47], %6 offset:256\n" \
  "ds_read_b64_tr_b16 v[40:41], %6 offset:200\nds_read_b64_tr_b16 v[42:43], %6 offset:192\nds_read_b64_tr_b16 v[36:37], %6 offset:136\nds_read_b64_tr_b16 v[38:39], %6 offset:128\n" \
  "ds_read_b64_tr_b16 v[32:33], %6 offset:72\nds_read_b64_tr_b16 v[34:35], %6 offset:64\nds_read_b64_tr_b16 v[28:29], %6 offset:8\nds_read_b64_tr_b16 v[30:31], %6 offset:0\n" \
  "ds_read_b64_tr_b16 v[88:89], %6 offset:968\nds_read_b64_tr_b16 v[90:91], %6 offset:960\nds_read_b64_tr_b16 v[84:85], %6 offset:904\nds_read_b64_tr_b16 v[86:87], %6 offset:896\n" \
  "ds_read_b64_tr_b16 v[80:81], %6 offset:840\nds_read_b64_tr_b16 v[82:83], %6 offset:832\nds_read_b64_tr_b16 v[76:77], %6 offset:776\nds_read_b64_tr_b16 v[78:79], %6 offset:768\n" \
  "ds_read_b64_tr_b16 v[72:73], %6 offset:712\nds_read_b64_tr_b16 v[74:75], %6 offset:704\nds_read_b64_tr_b16 v[68:69], %6 offset:648\nds_read_b64_tr_b16 v[70:71], %6 offset:640\n" \
  "ds_read_b64_tr_b16 v[64:65], %6 offset:584\nds_read_b64_tr_b16 v[66:67], %6 offset:576\nds_read_b64_tr_b16 v[60:61], %6 offset:520\nds_read_b64_tr_b16 v[62:63], %6 offset:512\n" \
  "ds_read_b64_tr_b16 v[92:93], %6 offset:1032\nds_read_b64_tr_b16 v[94:95], %6 offset:1024\nv_mov_b32 v237, 1\ns_waitcnt lgkmcnt(15)\nv_cmp_gt_u32 vcc, 2, v236\ns_cbranch_vccnz .LsU0%=\n.LbU0%=:\n" \
  "ds_read_b128 v[140:143], %9 offset:0\nds_read_b128 v[144:147], %9 offset:64\nds_read_b128 v[148:151], %9 offset:128\nds_read_b128 v[152:155], %9 offset:192\nds_read_b128 v[156:159], %9 offset:256\n" \
  "ds_read_b128 v[160:163], %9 offset:320\nds_read_b128 v[164:167], %9 offset:384\nds_read_b128 v[168:171], %9 offset:448\ns_waitcnt lgkmcnt(7)\n" \
  "v_mfma_f32_16x16x32_f16 v[220:223], v[56:59], v[140:143], 0\nds_read_b128 v[172:175], %9 offset:512\ns_waitcnt lgkmcnt(7)\nv_mfma_f32_16x16x32_f16 v[220:223], v[52:55], v[144:147], v[220:223]\n" \
  "ds_read_b128 v[176:179], %9 offset:576\ns_waitcnt lgkmcnt(7)\nv_mfma_f32_16x16x32_f16 v[220:223], v[48:51], v[148:151], v[220:223]\nds_read_b128 v[180:183], %9 offset:640\ns_waitcnt lgkmcnt(7)\n" \
  "v_mfma_f32_16x16x32_f16 v[220:223], v[44:47], v[152:155], v[220:223]\nds_read_b128 v[184:187], %9 offset:704\ns_waitcnt lgkmcnt(7)\n" \
  "v_mfma_f32_16x16x32_f16 v[220:223], v[40:43], v[156:159], v[220:223]\nds_read_b128 v[188:191], %9 offset:768\ns_waitcnt lgkmcnt(5)\n" \
  "v_mfma_f32_16x16x32_f16 v[220:223], v[36:39], v[160:163], v[220:223]\nds_read_b128 v[192:195], %9 offset:832\nv_mfma_f32_16x16x32_f16 v[220:223], v[32:35], v[164:167], v[220:223]\n" \
  "ds_read_b128 v[196:199], %9 offset:896\nv_mfma_f32_16x16x32_f16 v[220:223], v[28:31], v[168:171], v[220:223]\nds_read_b128 v[200:203], %9 offset:960\n" \
  "v_mfma_f32_16x16x32_f16 v[224:227], v[88:91], v[140:143], 0\nds_read_b64 v[104:105], %7 offset:64\nds_read_b64 v[106:107], %7 offset:72\n" \
  "v_mfma_f32_16x16x32_f16 v[224:227], v[84:87], v[144:147], v[224:227]\nds_read_b64 v[100:101], %7 offset:0\nds_read_b64 v[102:103], %7 offset:8\n" \
  "v_mfma_f32_16x16x32_f16 v[224:227], v[80:83], v[148:151], v[224:227]\nds_write_b128 %12, v[220:223] offset:0\nds_add_u32 %14, v237 offset:1024\n" \
  "v_mfma_f32_16x16x32_f16 v[224:227], v[76:79], v[152:155], v[224:227]\nds_read_b64 v[112:113], %7 offset:192\nds_read_b64 v[114:115], %7 offset:200\n" \
  "v_mfma_f32_16x16x32_f16 v[224:227], v[72:75], v[156:159], v[224:227]\nds_read_b64 v[120:121], %7 offset:320\nds_read_b64 v[122:123], %7 offset:328\n" \
  "v_mfma_f32_16x16x32_f16 v[224:227], v[68:71], v[160:163], v[224:227]\nds_read_b64 v[108:109], %7 offset:128\nds_read_b64 v[110:111], %7 offset:136\n" \
  "v_mfma_f32_16x16x32_f16 v[224:227], v[64:67], v[164:167], v[224:227]\nds_read_b64 v[116:117], %7 offset:256\nds_read_b64 v[118:119], %7 offset:264\n" \
  "v_mfma_f32_16x16x32_f16 v[224:227], v[60:63], v[168:171], v[224:227]\nds_read2_b32 v[124:125], %8 offset0:0 offset1:1\nv_mfma_f32_16x16x32_f16 v[228:231], v[92:95], v[168:171], 0\n" \
  "ds_read2_b32 v[126:127], %8 offset0:2 offset1:3\ns_waitcnt lgkmcnt(8)\nv_mfma_f32_16x16x32_f16 v[224:227], v[56:59], v[172:175], v[224:227]\nds_read_b64_tr_b16 v[96:97], %6 offset:1096\n" \
  "v_mfma_f32_16x16x32_f16 v[224:227], v[52:55], v[176:179], v[224:227]\nds_read_b64_tr_b16 v[98:99], %6 offset:1088\nv_mfma_f32_16x16x32_f16 v[224:227], v[48:51], v[180:183], v[224:227]\n" \
  "ds_read_b32 v236, %13\nv_mfma_f32_16x16x32_f16 v[224:227], v[44:47], v[184:187], v[224:227]\nds_read2_b32 v[128:129], %8 offset0:16 offset1:17\n" \
  "v_mfma_f32_16x16x32_f16 v[224:227], v[40:43], v[188:191], v[224:227]\nds_read2_b32 v[130:131], %8 offset0:18 offset1:19\nv_mfma_f32_16x16x32_f16 v[224:227], v[36:39], v[192:195], v[224:227]\n" \
  "ds_read2_b32 v[132:133], %8 offset0:32 offset1:33\nv_mfma_f32_16x16x32_f16 v[224:227], v[32:35], v[196:199], v[224:227]\nds_read2_b32 v[134:135], %8 offset0:34 offset1:35\n" \
  "v_mfma_f32_16x16x32_f16 v[224:227], v[28:31], v[200:203], v[224:227]\ns_waitcnt lgkmcnt(4)\nv_cmp_gt_u32 vcc, 3, v236\ns_cbranch_vccnz .LsU1%=\n.LbU1%=:\n" \
  "v_mfma_f32_16x16x32_f16 v[228:231], v[88:91], v[172:175], v[228:231]\nds_read_b128 v[140:143], %9 offset:1024\nv_mfma_f32_16x16x32_f16 v[228:231], v[84:87], v[176:179], v[228:231]\n" \
  "ds_read_b128 v[144:147], %9 offset:1088\nv_mfma_f32_16x16x32_f16 v[228:231], v[80:83], v[180:183], v[228:231]\nds_read_b128 v[148:151], %9 offset:1152\n" \
  "v_mfma_f32_16x16x32_f16 v[228:231], v[76:79], v[184:187], v[228:231]\nds_read_b32 v236, %13\nds_write_b128 %12, v[224:227] offset:1024\nds_add_u32 %14, v237 offset:1024\n" \
  "v_mfma_f32_16x16x32_f16 v[228:231], v[72:75], v[188:191], v[228:231]\nds_read_b128 v[152:155], %9 offset:1216\nv_mfma_f32_16x16x32_f16 v[228:231], v[68:71], v[192:195], v[228:231]\n" \
  "ds_read_b128 v[156:159], %9 offset:1280\nv_mfma_f32_16x16x32_f16 v[228:231], v[64:67], v[196:199], v[228:231]\nds_read_b128 v[160:163], %9 offset:1344\n" \
  "v_mfma_f32_16x16x32_f16 v[228:231], v[60:63], v[200:203], v[228:231]\nds_read_b128 v[164:167], %9 offset:1408\nv_mfma_f32_16x16x32_f16 v[232:235], v[92:95], v[200:203], 0\ns_waitcnt lgkmcnt(6)\n" \
  "v_cmp_gt_u32 vcc, 5, v236\ns_cbranch_vccnz .LsW0A%=\n.LbW0A%=:\nv_mfma_f32_16x16x32_f16 v[228:231], v[56:59], v[140:143], v[228:231]\nds_read_b128 v[168:171], %9 offset:1472\n" \
  "v_mfma_f32_16x16x32_f16 v[228:231], v[52:55], v[144:147], v[228:231]\nds_read_b64_tr_b16 v[204:205], %10 offset:32\nds_read_b64_tr_b16 v[206:207], %10 offset:0\n" \
  "v_mfma_f32_16x16x32_f16 v[228:231], v[48:51], v[148:151], v[228:231]\nds_read_b64_tr_b16 v[208:209], %10 offset:288\nds_read_b64_tr_b16 v[210:211], %10 offset:256\ns_waitcnt lgkmcnt(6)\n" \
  "v_mfma_f32_16x16x32_f16 v[228:231], v[44:47], v[152:155], v[228:231]\nds_read_b128 v[172:175], %9 offset:1536\nv_mfma_f32_16x16x32_f16 v[228:231], v[40:43], v[156:159], v[228:231]\n" \
  "ds_read_b128 v[176:179], %9 offset:1600\nv_mfma_f32_16x16x32_f16 v[228:231], v[36:39], v[160:163], v[228:231]\nds_read_b128 v[180:183], %9 offset:1664\ns_waitcnt lgkmcnt(7)\n" \
  "v_mfma_f32_16x16x32_f16 v[228:231], v[32:35], v[164:167], v[228:231]\nds_read_b128 v[184:187], %9 offset:1728\nv_mfma_f32_16x16x32_f16 v[228:231], v[28:31], v[168:171], v[228:231]\n" \
  "ds_read_b128 v[188:191], %9 offset:1792\ns_waitcnt lgkmcnt(5)\nv_mfma_f32_16x16x32_f16 v[228:231], v[204:207], v[104:107], v[228:231]\nds_read_b128 v[192:195], %9 offset:1856\n" \
  "v_mfma_f32_16x16x32_f16 v[228:231], v[208:211], v[100:103], v[228:231]\nds_read_b128 v[196:199], %9 offset:1920\nv_mfma_f32_16x16x32_f16 v[220:223], v[92:95], v[168:171], 0\n" \
  "ds_read_b128 v[200:203], %9 offset:1984\nv_mfma_f32_16x16x32_f16 v[232:235], v[204:207], v[112:115], v[232:235]\nds_read_b128 v[216:219], %9 offset:464\n" \
  "v_mfma_f32_16x16x32_f16 v[220:223], v[204:207], v[120:123], v[220:223]\nds_read_b64_tr_b16 v[204:205], %10 offset:544\nds_read_b64_tr_b16 v[206:207], %10 offset:512\n" \
  "v_mfma_f32_16x16x32_f16 v[232:235], v[208:211], v[108:111], v[232:235]\nds_write_b128 %12, v[228:231] offset:2048\nds_add_u32 %14, v237 offset:1024\n" \
  "v_mfma_f32_16x16x32_f16 v[220:223], v[208:211], v[116:119], v[220:223]\nds_read_b64_tr_b16 v[208:209], %10 offset:800\nds_read_b64_tr_b16 v[210:211], %10 offset:768\n" \
  "v_mfma_f32_16x16x32_f16 v[232:235], v[88:91], v[140:143], v[232:235]\nds_read_b128 v[140:143], %9 offset:2048\nv_mfma_f32_16x16x32_f16 v[232:235], v[84:87], v[144:147], v[232:235]\n" \
  "ds_read_b128 v[144:147], %9 offset:2112\nv_mfma_f32_16x16x32_f16 v[232:235], v[80:83], v[148:151], v[232:235]\nds_read_b128 v[148:151], %9 offset:2176\n" \
  "v_mfma_f32_16x16x32_f16 v[232:235], v[76:79], v[152:155], v[232:235]\nds_read_b128 v[152:155], %9 offset:2240\nv_mfma_f32_16x16x32_f16 v[232:235], v[72:75], v[156:159], v[232:235]\n" \
  "ds_read_b128 v[156:159], %9 offset:2304\nv_mfma_f32_16x16x32_f16 v[232:235], v[68:71], v[160:163], v[232:235]\nds_read_b128 v[160:163], %9 offset:2368\n" \
  "v_mfma_f32_16x16x32_f16 v[232:235], v[64:67], v[164:167], v[232:235]\nds_read_b128 v[164:167], %9 offset:2432\nv_mfma_f32_16x16x32_f16 v[232:235], v[60:63], v[168:171], v[232:235]\n" \
  "ds_read_b128 v[168:171], %9 offset:2496\ns_waitcnt lgkmcnt(5)\nv_mfma_f32_16x16x32_f16 v[232:235], v[56:59], v[172:175], v[232:235]\nds_read2_b32 v[136:137], %8 offset0:48 offset1:49\n" \
  "v_mfma_f32_16x16x32_f16 v[232:235], v[52:55], v[176:179], v[232:235]\nds_read2_b32 v[138:139], %8 offset0:50 offset1:51\nv_mfma_f32_16x16x32_f16 v[232:235], v[48:51], v[180:183], v[232:235]\n" \
  "v_mfma_f32_16x16x32_f16 v[232:235], v[44:47], v[184:187], v[232:235]\nv_mfma_f32_16x16x32_f16 v[232:235], v[40:43], v[188:191], v[232:235]\n" \
  "v_mfma_f32_16x16x32_f16 v[232:235], v[36:39], v[192:195], v[232:235]\nv_mfma_f32_16x16x32_f16 v[232:235], v[32:35], v[196:199], v[232:235]\n" \
  "v_mfma_f32_16x16x32_f16 v[232:235], v[28:31], v[200:203], v[232:235]\nv_mfma_f32_16x16x32_f16 v[232:235], v[204:207], v[104:107], v[232:235]\n" \
  "v_mfma_f32_16x16x32_f16 v[232:235], v[208:211], v[100:103], v[232:235]\nv_mfma_f32_16x16x32_f16 v[224:227], v[92:95], v[200:203], 0\n" \
  "v_mfma_f32_16x16x32_f16 v[220:223], v[204:207], v[112:115], v[220:223]\nv_mfma_f32_16x16x32_f16 v[224:227], v[204:207], v[120:123], v[224:227]\n" \
  "v_mfma_f32_16x16x32_f16 v[220:223], v[208:211], v[108:111], v[220:223]\nv_mfma_f32_16x16x32_f16 v[224:227], v[208:211], v[116:119], v[224:227]\n" \
  "v_mfma_f32_16x16x32_f16 v[220:223], v[88:91], v[172:175], v[220:223]\nds_read_b128 v[172:175], %9 offset:2560\nv_mfma_f32_16x16x32_f16 v[220:223], v[84:87], v[176:179], v[220:223]\n" \
  "ds_write_b128 %12, v[232:235] offset:3072\nds_add_u32 %14, v237 offset:1024\nv_mfma_f32_16x16x32_f16 v[220:223], v[80:83], v[180:183], v[220:223]\nds_read_b32 v236, %13\n" \
  "v_mfma_f32_16x16x32_f16 v[220:223], v[76:79], v[184:187], v[220:223]\nds_read_b128 v[176:179], %9 offset:2624\nv_mfma_f32_16x16x32_f16 v[220:223], v[72:75], v[188:191], v[220:223]\n" \
  "ds_read_b128 v[180:183], %9 offset:2688\nv_mfma_f32_16x16x32_f16 v[220:223], v[68:71], v[192:195], v[220:223]\nds_read_b128 v[184:187], %9 offset:2752\n" \
  "v_mfma_f32_16x16x32_f16 v[220:223], v[64:67], v[196:199], v[220:223]\nds_read_b128 v[188:191], %9 offset:2816\nv_mfma_f32_16x16x32_f16 v[220:223], v[60:63], v[200:203], v[220:223]\n" \
  "s_waitcnt lgkmcnt(4)\nv_cmp_gt_u32 vcc, 6, v236\ns_cbranch_vccnz .LsW1A%=\n.LbW1A%=:\nv_mfma_f32_16x16x32_f16 v[220:223], v[56:59], v[140:143], v[220:223]\n" \
  "ds_read_b64_tr_b16 v[204:205], %10 offset:1056\nds_read_b64_tr_b16 v[206:207], %10 offset:1024\nv_mfma_f32_16x16x32_f16 v[220:223], v[52:55], v[144:147], v[220:223]\n" \
  "ds_read_b64_tr_b16 v[208:209], %10 offset:1312\nds_read_b64_tr_b16 v[210:211], %10 offset:1280\nv_mfma_f32_16x16x32_f16 v[220:223], v[48:51], v[148:151], v[220:223]\n" \
  "ds_read_b64_tr_b16 v[212:213], %11 offset:64\nds_read_b64_tr_b16 v[214:215], %11 offset:0\nv_mfma_f32_16x16x32_f16 v[220:223], v[44:47], v[152:155], v[220:223]\n" \
  "ds_read_b128 v[192:195], %9 offset:2880\nv_mfma_f32_16x16x32_f16 v[220:223], v[40:43], v[156:159], v[220:223]\nds_read_b128 v[196:199], %9 offset:2944\n" \
  "v_mfma_f32_16x16x32_f16 v[220:223], v[36:39], v[160:163], v[220:223]\nds_read_b128 v[200:203], %9 offset:3008\nv_mfma_f32_16x16x32_f16 v[220:223], v[32:35], v[164:167], v[220:223]\n" \
  "v_mfma_f32_16x16x32_f16 v[220:223], v[28:31], v[168:171], v[220:223]\ns_waitcnt lgkmcnt(3)\nv_mfma_f32_16x16x32_f16 v[220:223], v[204:207], v[104:107], v[220:223]\n" \
  "v_mfma_f32_16x16x32_f16 v[220:223], v[208:211], v[100:103], v[220:223]\nv_mfma_f32_16x16x32_f16 v[220:223], v[212:215], v[124:127], v[220:223]\n" \
  "v_mfma_f32_16x16x32_f16 v[220:223], v[96:99], v[216:219], v[220:223]\nds_read_b128 v[216:219], %9 offset:976\nv_mfma_f32_16x16x32_f16 v[228:231], v[92:95], v[168:171], 0\n" \
  "v_mfma_f32_16x16x32_f16 v[224:227], v[204:207], v[112:115], v[224:227]\nv_mfma_f32_16x16x32_f16 v[228:231], v[204:207], v[120:123], v[228:231]\nds_read_b64_tr_b16 v[204:205], %10 offset:1568\n" \
  "ds_read_b64_tr_b16 v[206:207], %10 offset:1536\nv_mfma_f32_16x16x32_f16 v[224:227], v[208:211], v[108:111], v[224:227]\nv_mfma_f32_16x16x32_f16 v[228:231], v[208:211], v[116:119], v[228:231]\n" \
  "ds_write_b128 %12, v[220:223] offset:4096\nds_add_u32 %14, v237 offset:1024\nv_mfma_f32_16x16x32_f16 v[224:227], v[212:215], v[128:131], v[224:227]\nds_read_b64_tr_b16 v[208:209], %10 offset:1824\n" \
  "ds_read_b64_tr_b16 v[210:211], %10 offset:1792\nv_mfma_f32_16x16x32_f16 v[228:231], v[212:215], v[132:135], v[228:231]\nv_mfma_f32_16x16x32_f16 v[232:235], v[212:215], v[136:139], 0\n" \
  "ds_read_b64_tr_b16 v[212:213], %11 offset:576\nds_read_b64_tr_b16 v[214:215], %11 offset:512\nv_mfma_f32_16x16x32_f16 v[224:227], v[88:91], v[140:143], v[224:227]\n" \
  "v_mfma_f32_16x16x32_f16 v[224:227], v[84:87], v[144:147], v[224:227]\nv_mfma_f32_16x16x32_f16 v[224:227], v[80:83], v[148:151], v[224:227]\n" \
  "v_mfma_f32_16x16x32_f16 v[224:227], v[76:79], v[152:155], v[224:227]\nv_mfma_f32_16x16x32_f16 v[224:227], v[72:75], v[156:159], v[224:227]\n" \
  "v_mfma_f32_16x16x32_f16 v[224:227], v[68:71], v[160:163], v[224:227]\nv_mfma_f32_16x16x32_f16 v[224:227], v[64:67], v[164:167], v[224:227]\n" \
  "v_mfma_f32_16x16x32_f16 v[224:227], v[60:63], v[168:171], v[224:227]\nv_mfma_f32_16x16x32_f16 v[224:227], v[56:59], v[172:175], v[224:227]\n" \
  "v_mfma_f32_16x16x32_f16 v[224:227], v[52:55], v[176:179], v[224:227]\nv_mfma_f32_16x16x32_f16 v[224:227], v[48:51], v[180:183], v[224:227]\n" \
  "v_mfma_f32_16x16x32_f16 v[224:227], v[44:47], v[184:187], v[224:227]\nv_mfma_f32_16x16x32_f16 v[224:227], v[40:43], v[188:191], v[224:227]\ns_waitcnt lgkmcnt(0)\n" \
  "v_mfma_f32_16x16x32_f16 v[224:227], v[36:39], v[192:195], v[224:227]\nv_mfma_f32_16x16x32_f16 v[224:227], v[32:35], v[196:199], v[224:227]\n" \
  "v_mfma_f32_16x16x32_f16 v[224:227], v[28:31], v[200:203], v[224:227]\ns_cmp_lg_u32 %19, 0\ns_cbranch_scc1 .Lp%=\nbuffer_load_dwordx2 v[254:255], %17, %18, 0 offen sc1\n" \
  "buffer_load_dwordx4 v[238:241], %15, %18, 0 offen sc1\nbuffer_load_dwordx4 v[242:245], %16, %18, 0 offen sc1\n.Lp%=:\nv_mfma_f32_16x16x32_f16 v[224:227], v[204:207], v[104:107], v[224:227]\n" \
  "v_mfma_f32_16x16x32_f16 v[224:227], v[208:211], v[100:103], v[224:227]\nv_mfma_f32_16x16x32_f16 v[224:227], v[212:215], v[124:127], v[224:227]\n" \
  "v_mfma_f32_16x16x32_f16 v[224:227], v[96:99], v[216:219], v[224:227]\nds_read_b32 v236, %13\nv_mfma_f32_16x16x32_f16 v[232:235], v[92:95], v[200:203], v[232:235]\n" \
  "ds_read_b128 v[216:219], %9 offset:1488\nv_mfma_f32_16x16x32_f16 v[228:231], v[204:207], v[112:115], v[228:231]\nv_mfma_f32_16x16x32_f16 v[232:235], v[204:207], v[120:123], v[232:235]\n" \
  "v_mfma_f32_16x16x32_f16 v[228:231], v[208:211], v[108:111], v[228:231]\nv_mfma_f32_16x16x32_f16 v[232:235], v[208:211], v[116:119], v[232:235]\ns_waitcnt lgkmcnt(1)\nv_cmp_gt_u32 vcc, 7, v236\n" \
  "s_cbranch_vccnz .LsU3%=\n.LbU3%=:\nds_write_b128 %12, v[224:227] offset:5120\nds_add_u32 %14, v237 offset:1024\nv_mfma_f32_16x16x32_f16 v[228:231], v[212:215], v[128:131], v[228:231]\n" \
  "ds_read_b128 v[140:143], %9 offset:3072\nv_mfma_f32_16x16x32_f16 v[232:235], v[212:215], v[132:135], v[232:235]\nds_read_b128 v[144:147], %9 offset:3136\n" \
  "v_mfma_f32_16x16x32_f16 v[228:231], v[88:91], v[172:175], v[228:231]\nds_read_b128 v[148:151], %9 offset:3200\nv_mfma_f32_16x16x32_f16 v[228:231], v[84:87], v[176:179], v[228:231]\n" \
  "ds_read_b128 v[152:155], %9 offset:3264\nv_mfma_f32_16x16x32_f16 v[228:231], v[80:83], v[180:183], v[228:231]\nds_read_b32 v236, %13\n" \
  "v_mfma_f32_16x16x32_f16 v[228:231], v[76:79], v[184:187], v[228:231]\nds_read_b128 v[156:159], %9 offset:3328\nv_mfma_f32_16x16x32_f16 v[228:231], v[72:75], v[188:191], v[228:231]\n" \
  "ds_read_b128 v[160:163], %9 offset:3392\nv_mfma_f32_16x16x32_f16 v[228:231], v[68:71], v[192:195], v[228:231]\nds_read_b128 v[164:167], %9 offset:3456\n" \
  "v_mfma_f32_16x16x32_f16 v[228:231], v[64:67], v[196:199], v[228:231]\nds_read_b128 v[168:171], %9 offset:3520\nv_mfma_f32_16x16x32_f16 v[228:231], v[60:63], v[200:203], v[228:231]\n" \
  "s_waitcnt lgkmcnt(4)\nv_cmp_gt_u32 vcc, 8, v236\ns_cbranch_vccnz .LsALL%=\n.LbALL%=:\nv_mfma_f32_16x16x32_f16 v[228:231], v[56:59], v[140:143], v[228:231]\n" \
  "ds_read_b64_tr_b16 v[204:205], %10 offset:2080\nds_read_b64_tr_b16 v[206:207], %10 offset:2048\nv_mfma_f32_16x16x32_f16 v[228:231], v[52:55], v[144:147], v[228:231]\n" \
  "ds_read_b64_tr_b16 v[208:209], %10 offset:2336\nds_read_b64_tr_b16 v[210:211], %10 offset:2304\nv_mfma_f32_16x16x32_f16 v[228:231], v[48:51], v[148:151], v[228:231]\n" \
  "ds_read_b64_tr_b16 v[212:213], %11 offset:1088\nds_read_b64_tr_b16 v[214:215], %11 offset:1024\nv_mfma_f32_16x16x32_f16 v[228:231], v[44:47], v[152:155], v[228:231]\n" \
  "ds_read_b128 v[172:175], %9 offset:3584\ns_waitcnt lgkmcnt(7)\nv_mfma_f32_16x16x32_f16 v[228:231], v[40:43], v[156:159], v[228:231]\nds_read_b128 v[176:179], %9 offset:3648\n" \
  "v_mfma_f32_16x16x32_f16 v[228:231], v[36:39], v[160:163], v[228:231]\nds_read_b128 v[180:183], %9 offset:3712\nv_mfma_f32_16x16x32_f16 v[228:231], v[32:35], v[164:167], v[228:231]\n" \
  "ds_read_b128 v[184:187], %9 offset:3776\nv_mfma_f32_16x16x32_f16 v[228:231], v[28:31], v[168:171], v[228:231]\nds_read_b128 v[188:191], %9 offset:3840\ns_waitcnt lgkmcnt(5)\n" \
  "v_mfma_f32_16x16x32_f16 v[228:231], v[204:207], v[104:107], v[228:231]\nds_read_b128 v[192:195], %9 offset:3904\nv_mfma_f32_16x16x32_f16 v[228:231], v[208:211], v[100:103], v[228:231]\n" \
  "ds_read_b128 v[196:199], %9 offset:3968\nv_mfma_f32_16x16x32_f16 v[228:231], v[212:215], v[124:127], v[228:231]\ns_cmp_lg_u32 %19, 0\ns_cbranch_scc1 .Lq%=\n" \
  "buffer_load_dwordx4 v[246:249], %15, %18, 0 offen sc1\nbuffer_load_dwordx4 v[250:253], %16, %18, 0 offen sc1\n.Lq%=:\nv_mfma_f32_16x16x32_f16 v[228:231], v[96:99], v[216:219], v[228:231]\n" \
  "ds_read_b128 v[200:203], %9 offset:4032\nv_mfma_f32_16x16x32_f16 v[232:235], v[204:207], v[112:115], v[232:235]\nds_read_b64_tr_b16 v[204:205], %10 offset:2592\n" \
  "ds_read_b64_tr_b16 v[206:207], %10 offset:2560\nv_mfma_f32_16x16x32_f16 v[232:235], v[208:211], v[108:111], v[232:235]\nds_read_b64_tr_b16 v[208:209], %10 offset:2848\n" \
  "ds_read_b64_tr_b16 v[210:211], %10 offset:2816\nv_mfma_f32_16x16x32_f16 v[232:235], v[212:215], v[128:131], v[232:235]\nds_write_b128 %12, v[228:231] offset:6144\nds_add_u32 %14, v237 offset:1024\n" \
  "v_mfma_f32_16x16x32_f16 v[232:235], v[88:91], v[140:143], v[232:235]\nds_read_b64_tr_b16 v[212:213], %11 offset:1600\nds_read_b64_tr_b16 v[214:215], %11 offset:1536\n" \
  "v_mfma_f32_16x16x32_f16 v[232:235], v[84:87], v[144:147], v[232:235]\nds_read_b128 v[216:219], %9 offset:2000\nv_mfma_f32_16x16x32_f16 v[232:235], v[80:83], v[148:151], v[232:235]\n" \
  "v_mfma_f32_16x16x32_f16 v[232:235], v[76:79], v[152:155], v[232:235]\nv_mfma_f32_16x16x32_f16 v[232:235], v[72:75], v[156:159], v[232:235]\n" \
  "v_mfma_f32_16x16x32_f16 v[232:235], v[68:71], v[160:163], v[232:235]\nv_mfma_f32_16x16x32_f16 v[232:235], v[64:67], v[164:167], v[232:235]\n" \
  "v_mfma_f32_16x16x32_f16 v[232:235], v[60:63], v[168:171], v[232:235]\ns_waitcnt lgkmcnt(0)\nv_mfma_f32_16x16x32_f16 v[232:235], v[56:59], v[172:175], v[232:235]\n" \
  "v_mfma_f32_16x16x32_f16 v[232:235], v[52:55], v[176:179], v[232:235]\nv_mfma_f32_16x16x32_f16 v[232:235], v[48:51], v[180:183], v[232:235]\n" \
  "v_mfma_f32_16x16x32_f16 v[232:235], v[44:47], v[184:187], v[232:235]\nv_mfma_f32_16x16x32_f16 v[232:235], v[40:43], v[188:191], v[232:235]\n" \
  "v_mfma_f32_16x16x32_f16 v[232:235], v[36:39], v[192:195], v[232:235]\nv_mfma_f32_16x16x32_f16 v[232:235], v[32:35], v[196:199], v[232:235]\n" \
  "v_mfma_f32_16x16x32_f16 v[232:235], v[28:31], v[200:203], v[232:235]\nv_mfma_f32_16x16x32_f16 v[232:235], v[204:207], v[104:107], v[232:235]\n" \
  "v_mfma_f32_16x16x32_f16 v[232:235], v[208:211], v[100:103], v[232:235]\nv_mfma_f32_16x16x32_f16 v[232:235], v[212:215], v[124:127], v[232:235]\n" \
  "v_mfma_f32_16x16x32_f16 v[232:235], v[96:99], v[216:219], v[232:235]\ns_nop 7\nds_write_b128 %12, v[232:235] offset:7168\nds_add_u32 %14, v237 offset:1024\ns_branch .Le%=\n.LsU0%=:\n" \
  "s_mov_b32 %5, %20\n.LlU0%=:\nds_read_b32 v236, %13\ns_sub_u32 %5, %5, 1\ns_waitcnt lgkmcnt(0)\nv_cmp_gt_u32 vcc, 2, v236\ns_cbranch_scc1 .LbU0%=\ns_cbranch_vccnz .LlU0%=\ns_branch .LbU0%=\n.LsU1%=:\n" \
  "s_mov_b32 %5, %20\n.LlU1%=:\nds_read_b32 v236, %13\ns_sub_u32 %5, %5, 1\ns_waitcnt lgkmcnt(0)\nv_cmp_gt_u32 vcc, 3, v236\ns_cbranch_scc1 .LbU1%=\ns_cbranch_vccnz .LlU1%=\ns_branch .LbU1%=\n.LsW0A%=:\n" \
  "s_mov_b32 %5, %20\n.LlW0A%=:\nds_read_b32 v236, %13\ns_sub_u32 %5, %5, 1\ns_waitcnt lgkmcnt(0)\nv_cmp_gt_u32 vcc, 5, v236\ns_cbranch_scc1 .LbW0A%=\ns_cbranch_vccnz .LlW0A%=\ns_branch .LbW0A%=\n" \
  ".LsW1A%=:\ns_mov_b32 %5, %20\n.LlW1A%=:\nds_read_b32 v236, %13\ns_sub_u32 %5, %5, 1\ns_waitcnt lgkmcnt(0)\nv_cmp_gt_u32 vcc, 6, v236\ns_cbranch_scc1 .LbW1A%=\ns_cbranch_vccnz .LlW1A%=\n" \
  "s_branch .LbW1A%=\n.LsU3%=:\ns_mov_b32 %5, %20\n.LlU3%=:\nds_read_b32 v236, %13\ns_sub_u32 %5, %5, 1\ns_waitcnt lgkmcnt(0)\nv_cmp_gt_u32 vcc, 7, v236\ns_cbranch_scc1 .LbU3%=\ns_cbranch_vccnz .LlU3%=\n" \
  "s_branch .LbU3%=\n.LsALL%=:\ns_mov_b32 %5, %20\n.LlALL%=:\nds_read_b32 v236, %13\ns_sub_u32 %5, %5, 1\ns_waitcnt lgkmcnt(0)\nv_cmp_gt_u32 vcc, 8, v236\ns_cbranch_scc1 .LbALL%=\n" \
  "s_cbranch_vccnz .LlALL%=\ns_branch .LbALL%=\n.Le%=:\n" MFT "s_cmp_lg_u32 %19, 0\ns_cbranch_scc1 .Lz%=\ns_mov_b32 %5, %20\n.Lr%=:\ns_waitcnt vmcnt(2)\nv_readlane_b32 vcc_lo, v239, 0\n" \
  "v_readlane_b32 vcc_hi, v239, 1\nv_mov_b32 v32, v238\nv_mov_b32 v33, v240\nv_mov_b32 v28, vcc_lo\nv_mov_b32 v29, vcc_hi\ns_mov_b32 vcc_lo, 0xaaaaaaaa\ns_mov_b32 vcc_hi, vcc_lo\nv_mov_b32 v34, v242\n" \
  "v_cndmask_b32 v28, v28, v29, vcc\nv_mov_b32 v35, v244\nv_xor_b32 v29, v239, v28\nv_xor_b32 v30, v241, v28\nv_xor_b32 v31, v243, v28\nv_or3_b32 v29, v29, v30, v31\nv_xor_b32 v30, v245, v28\n" \
  "v_xor_b32 v31, %21, v28\nv_and_b32 v31, 0x1fffff, v31\nv_or3_b32 v29, v29, v30, v31\nv_cmp_ne_u32 vcc, 0, v29\ns_cbranch_vccz .Lz%=\nbuffer_load_dwordx4 v[238:241], %15, %18, 0 offen sc1\n" \
  "buffer_load_dwordx4 v[242:245], %16, %18, 0 offen sc1\ns_waitcnt vmcnt(2)\nv_readlane_b32 vcc_lo, v247, 0\nv_readlane_b32 vcc_hi, v247, 1\nv_mov_b32 v32, v246\nv_mov_b32 v33, v248\n" \
  "v_mov_b32 v28, vcc_lo\nv_mov_b32 v29, vcc_hi\ns_mov_b32 vcc_lo, 0xaaaaaaaa\ns_mov_b32 vcc_hi, vcc_lo\nv_mov_b32 v34, v250\nv_cndmask_b32 v28, v28, v29, vcc\nv_mov_b32 v35, v252\n" \
  "v_xor_b32 v29, v247, v28\nv_xor_b32 v30, v249, v28\nv_xor_b32 v31, v251, v28\nv_or3_b32 v29, v29, v30, v31\nv_xor_b32 v30, v253, v28\nv_xor_b32 v31, %21, v28\nv_and_b32 v31, 0x1fffff, v31\n" \
  "v_or3_b32 v29, v29, v30, v31\nv_cmp_ne_u32 vcc, 0, v29\ns_cbranch_vccz .Lz%=\nbuffer_load_dwordx4 v[246:249], %15, %18, 0 offen sc1\nbuffer_load_dwordx4 v[250:253], %16, %18, 0 offen sc1\n" \
  "s_sub_u32 %5, %5, 32\ns_cbranch_scc0 .Lr%=\n.Lz%=:\nv_mov_b64 v[36:37], v[254:255]\n"
#define HP_CLOB "v80", "v81", "v82", "v83", "v84", "v85", "v86", "v87", "v88", "v89", "v90", "v91", "v92", "v93", "v94", "v95", "v96", "v97", "v98", "v99", "v100", "v101", "v102", "v103", "v104", "v105", "v106", "v107", "v108", "v109", "v110", "v111", "v112", "v113", "v114", "v115", "v116", "v117", "v118", "v119", "v120", "v121", "v122", "v123", "v124", "v125", "v126", "v127", "v128", "v129", "v130", "v131", "v132", "v133", "v134", "v135", "v136", "v137", "v138", "v139", "v140", "v141", "v142", "v143", "v144", "v145", "v146"
#define HP_ASM \
  "v_mov_b32 v146, 1\nv_cmp_ne_u32 vcc, %[o16], v65\nv_cmp_ne_u32 %[o5], %[o16], v67\ns_or_b64 vcc, vcc, %[o5]\ns_cbranch_scc1 .Lsp0%=\n.Lbp0%=:\nv_and_b32 v116, 0xffff0000, v64\n" \
  "v_and_b32 v117, 0xffff0000, v66\nv_rcp_f32 v116, v116\nv_rcp_f32 v117, v117\nv_lshlrev_b32 v118, 16, v64\nv_lshlrev_b32 v119, 16, v66\nv_mul_f32 v118, v118, v116\nv_mul_f32 v119, v119, v117\n" \
  "v_fma_mixlo_f16 v120, v32, v116, -v118\nv_fma_mixlo_f16 v121, v34, v116, -v118\nv_fma_mixlo_f16 v122, v36, v116, -v118\nv_fma_mixlo_f16 v123, v38, v116, -v118\nv_fma_mixhi_f16 v120, v33, v117, -v119\n" \
  "v_fma_mixhi_f16 v121, v35, v117, -v119\nv_fma_mixhi_f16 v122, v37, v117, -v119\nv_fma_mixhi_f16 v123, v39, v117, -v119\nds_write_addtid_b32 v120 offset:0\nds_write_addtid_b32 v121 offset:4576\n" \
  "ds_write_addtid_b32 v122 offset:9152\nds_write_addtid_b32 v123 offset:13728\nds_add_u32 %[o13], v146\n" HST(4) \
  "v_cmp_ne_u32 vcc, %[o16], v69\nv_cmp_ne_u32 %[o5], %[o16], v71\ns_or_b64 vcc, vcc, %[o5]\ns_cbranch_scc1 .Lsp1%=\n.Lbp1%=:\nv_and_b32 v116, 0xffff0000, v68\nv_and_b32 v117, 0xffff0000, v70\n" \
  "v_rcp_f32 v116, v116\nv_rcp_f32 v117, v117\nv_lshlrev_b32 v118, 16, v68\nv_lshlrev_b32 v119, 16, v70\nv_mul_f32 v118, v118, v116\nv_mul_f32 v119, v119, v117\nv_fma_mixlo_f16 v120, v40, v116, -v118\n" \
  "v_fma_mixlo_f16 v121, v42, v116, -v118\nv_fma_mixlo_f16 v122, v44, v116, -v118\nv_fma_mixlo_f16 v123, v46, v116, -v118\nv_fma_mixhi_f16 v120, v41, v117, -v119\nv_fma_mixhi_f16 v121, v43, v117, -v119\n" \
  "v_fma_mixhi_f16 v122, v45, v117, -v119\nv_fma_mixhi_f16 v123, v47, v117, -v119\nds_write_addtid_b32 v120 offset:1024\nds_write_addtid_b32 v121 offset:5600\nds_write_addtid_b32 v122 offset:10176\n" \
  "ds_write_addtid_b32 v123 offset:14752\nds_add_u32 %[o13], v146\nds_read_b32 v144, %[o12]\nds_read_b128 v[80:83], %[o9] offset:0\nds_read_b128 v[84:87], %[o9] offset:16\nv_cmp_ne_u32 vcc, %[o16], v73\n" \
  "v_cmp_ne_u32 %[o5], %[o16], v75\ns_or_b64 vcc, vcc, %[o5]\ns_cbranch_scc1 .Lsp2%=\n.Lbp2%=:\nv_and_b32 v116, 0xffff0000, v72\nv_and_b32 v117, 0xffff0000, v74\nv_rcp_f32 v116, v116\n" \
  "v_rcp_f32 v117, v117\nv_lshlrev_b32 v118, 16, v72\nv_lshlrev_b32 v119, 16, v74\nv_mul_f32 v118, v118, v116\nv_mul_f32 v119, v119, v117\nv_fma_mixlo_f16 v120, v48, v116, -v118\n" \
  "v_fma_mixlo_f16 v121, v50, v116, -v118\nv_fma_mixlo_f16 v122, v52, v116, -v118\nv_fma_mixlo_f16 v123, v54, v116, -v118\nv_fma_mixhi_f16 v120, v49, v117, -v119\nv_fma_mixhi_f16 v121, v51, v117, -v119\n" \
  "v_fma_mixhi_f16 v122, v53, v117, -v119\nv_fma_mixhi_f16 v123, v55, v117, -v119\nds_write_addtid_b32 v120 offset:2048\nds_write_addtid_b32 v121 offset:6624\nds_write_addtid_b32 v122 offset:11200\n" \
  "ds_write_addtid_b32 v123 offset:15776\nds_add_u32 %[o13], v146\n" HPRIO \
  "s_waitcnt lgkmcnt(5)\nv_cmp_gt_u32 vcc, 2, v144\ns_cbranch_vccnz .Lsw0%=\n.Lbw0%=:\nds_read_b32 v144, %[o12]\nds_read_b128 v[88:91], %[o9] offset:1024\nds_read_b128 v[92:95], %[o9] offset:1040\n" \
  "ds_read_b128 v[136:139], %[o23]\nds_read_b128 v[140:143], %[o23] offset:16\nv_alignbit_b32 v97, v82, v81, 16\nv_alignbit_b32 v98, v83, v82, 16\nv_alignbit_b32 v99, v84, v83, 16\n" \
  "v_alignbit_b32 v100, v85, v84, 16\nv_alignbit_b32 v101, v86, v85, 16\nv_pk_add_f16 v108, v81, v83\nv_pk_add_f16 v109, v82, v84\nv_pk_add_f16 v110, v83, v85\nv_pk_add_f16 v111, v84, v86\n" \
  "v_pk_add_f16 v104, v97, v98\nv_pk_add_f16 v105, v98, v99\nv_pk_add_f16 v106, v99, v100\nv_pk_add_f16 v107, v100, v101\nv_pk_add_f16 v104, v104, v82\nv_pk_add_f16 v105, v105, v83\n" \
  "v_pk_add_f16 v106, v106, v84\nv_pk_add_f16 v107, v107, v85\nds_write_b128 %[o10], v[104:107] offset:32\nv_pk_add_f16 v108, v108, v104\nv_pk_add_f16 v109, v109, v105\nv_pk_add_f16 v110, v110, v106\n" \
  "v_pk_add_f16 v111, v111, v107\nds_write_b128 %[o10], v[108:111] offset:3232\nds_add_u32 %[o13], v146\n" HST(5) \
  "v_alignbit_b32 v96, v81, v80, 16\nv_alignbit_b32 v102, v87, v86, 16\nv_pk_add_f16 v112, v80, v84\nv_pk_add_f16 v113, v81, v85\nv_pk_add_f16 v114, v82, v86\nv_pk_add_f16 v115, v83, v87\n" \
  "v_pk_add_f16 v104, v96, v99\nv_pk_add_f16 v105, v97, v100\nv_pk_add_f16 v106, v98, v101\nv_pk_add_f16 v107, v99, v102\nv_pk_add_f16 v104, v104, v108\nv_pk_add_f16 v105, v105, v109\n" \
  "v_pk_add_f16 v106, v106, v110\nv_pk_add_f16 v107, v107, v111\nds_write_b128 %[o10], v[104:107] offset:6336\nv_pk_add_f16 v112, v112, v104\nv_pk_add_f16 v113, v113, v105\n" \
  "v_pk_add_f16 v114, v114, v106\nv_pk_add_f16 v115, v115, v107\nds_write_b128 %[o10], v[112:115] offset:8464\ns_waitcnt lgkmcnt(7)\nv_cmp_gt_u32 vcc, 3, v144\ns_cbranch_vccnz .Lsw1%=\n.Lbw1%=:\n" \
  "v_alignbit_b32 v97, v90, v89, 16\nv_alignbit_b32 v98, v91, v90, 16\nv_alignbit_b32 v99, v92, v91, 16\nv_alignbit_b32 v100, v93, v92, 16\nv_alignbit_b32 v101, v94, v93, 16\n" \
  "v_pk_add_f16 v108, v89, v91\nv_pk_add_f16 v109, v90, v92\nv_pk_add_f16 v110, v91, v93\nv_pk_add_f16 v111, v92, v94\nv_pk_add_f16 v104, v97, v98\nv_pk_add_f16 v105, v98, v99\n" \
  "v_pk_add_f16 v106, v99, v100\nv_pk_add_f16 v107, v100, v101\nv_pk_add_f16 v104, v104, v90\nv_pk_add_f16 v105, v105, v91\nv_pk_add_f16 v106, v106, v92\nv_pk_add_f16 v107, v107, v93\n" \
  "ds_write_b128 %[o10], v[104:107] offset:1056\nv_pk_add_f16 v108, v108, v104\nv_pk_add_f16 v109, v109, v105\nv_pk_add_f16 v110, v110, v106\nv_pk_add_f16 v111, v111, v107\n" \
  "ds_write_b128 %[o10], v[108:111] offset:4256\nds_add_u32 %[o13], v146\nds_read_b32 v145, %[o12] offset:1024\nds_read_b64 v[124:125], %[o11] offset:0\nds_read_b64 v[126:127], %[o11] offset:9216\n" \
  "ds_read_b64 v[128:129], %[o11] offset:18432\nds_read_b64 v[130:131], %[o11] offset:27648\nv_cmp_ne_u32 vcc, %[o16], v77\nv_cmp_ne_u32 %[o5], %[o16], v79\ns_or_b64 vcc, vcc, %[o5]\n" \
  "s_cbranch_scc1 .Lsp3%=\n.Lbp3%=:\nv_and_b32 v116, 0xffff0000, v76\nv_and_b32 v117, 0xffff0000, v78\nv_rcp_f32 v116, v116\nv_rcp_f32 v117, v117\nv_lshlrev_b32 v118, 16, v76\n" \
  "v_lshlrev_b32 v119, 16, v78\nv_mul_f32 v118, v118, v116\nv_mul_f32 v119, v119, v117\nv_fma_mixlo_f16 v120, v56, v116, -v118\nv_fma_mixlo_f16 v121, v58, v116, -v118\n" \
  "v_fma_mixlo_f16 v122, v60, v116, -v118\nv_fma_mixlo_f16 v123, v62, v116, -v118\nv_fma_mixhi_f16 v120, v57, v117, -v119\nv_fma_mixhi_f16 v121, v59, v117, -v119\nv_fma_mixhi_f16 v122, v61, v117, -v119\n" \
  "v_fma_mixhi_f16 v123, v63, v117, -v119\nds_write_addtid_b32 v120 offset:3072\nds_write_addtid_b32 v121 offset:7648\nds_write_addtid_b32 v122 offset:12224\nds_write_addtid_b32 v123 offset:16800\n" \
  "ds_add_u32 %[o13], v146\nds_read_b32 v144, %[o12]\nds_read_b128 v[80:83], %[o9] offset:2048\nds_read_b128 v[84:87], %[o9] offset:2064\ns_waitcnt lgkmcnt(8)\nv_cmp_gt_u32 vcc, %[o6], v145\n" \
  "s_cbranch_vccnz .Lss0%=\n.Lbs0%=:\nv_pk_fma_f32 v[124:125], v[124:125], v[136:137], v[32:33]\nv_pk_fma_f32 v[126:127], v[126:127], v[138:139], v[34:35]\n" \
  "v_pk_fma_f32 v[128:129], v[128:129], v[140:141], v[36:37]\nv_pk_fma_f32 v[130:131], v[130:131], v[142:143], v[38:39]\ns_add_u32 %[o6], %[o6], 2\nv_cvt_pk_bf16_f32 v132, v124, v126\n" \
  "v_cvt_pk_bf16_f32 v133, v128, v130\nv_cvt_pk_bf16_f32 v134, v125, v127\nv_cvt_pk_bf16_f32 v135, v129, v131\nglobal_store_dwordx2 %[o14], v[132:133], %[o18]\n" \
  "global_store_dwordx2 %[o14], v[134:135], %[o18] offset:1024\n" HST(7) \
  "v_alignbit_b32 v96, v89, v88, 16\nv_alignbit_b32 v102, v95, v94, 16\nv_pk_add_f16 v112, v88, v92\nv_pk_add_f16 v113, v89, v93\nv_pk_add_f16 v114, v90, v94\nv_pk_add_f16 v115, v91, v95\n" \
  "v_pk_add_f16 v104, v96, v99\nv_pk_add_f16 v105, v97, v100\nv_pk_add_f16 v106, v98, v101\nv_pk_add_f16 v107, v99, v102\nv_pk_add_f16 v104, v104, v108\nv_pk_add_f16 v105, v105, v109\n" \
  "v_pk_add_f16 v106, v106, v110\nv_pk_add_f16 v107, v107, v111\nds_write_b128 %[o10], v[104:107] offset:7360\nv_pk_add_f16 v112, v112, v104\nv_pk_add_f16 v113, v113, v105\n" \
  "v_pk_add_f16 v114, v114, v106\nv_pk_add_f16 v115, v115, v107\nds_write_b128 %[o10], v[112:115] offset:9488\nds_read_b32 v145, %[o12] offset:1024\nds_read_b64 v[124:125], %[o11] offset:2048\n" \
  "ds_read_b64 v[126:127], %[o11] offset:11264\nds_read_b64 v[128:129], %[o11] offset:20480\nds_read_b64 v[130:131], %[o11] offset:29696\ns_waitcnt lgkmcnt(7)\nv_cmp_gt_u32 vcc, 4, v144\n" \
  "s_cbranch_vccnz .Lsw2%=\n.Lbw2%=:\nv_alignbit_b32 v97, v82, v81, 16\nv_alignbit_b32 v98, v83, v82, 16\nv_alignbit_b32 v99, v84, v83, 16\nv_alignbit_b32 v100, v85, v84, 16\n" \
  "v_alignbit_b32 v101, v86, v85, 16\nv_pk_add_f16 v108, v81, v83\nv_pk_add_f16 v109, v82, v84\nv_pk_add_f16 v110, v83, v85\nv_pk_add_f16 v111, v84, v86\nv_pk_add_f16 v104, v97, v98\n" \
  "v_pk_add_f16 v105, v98, v99\nv_pk_add_f16 v106, v99, v100\nv_pk_add_f16 v107, v100, v101\nv_pk_add_f16 v104, v104, v82\nv_pk_add_f16 v105, v105, v83\nv_pk_add_f16 v106, v106, v84\n" \
  "v_pk_add_f16 v107, v107, v85\nds_write_b128 %[o10], v[104:107] offset:2080\nv_pk_add_f16 v108, v108, v104\nv_pk_add_f16 v109, v109, v105\nv_pk_add_f16 v110, v110, v106\nv_pk_add_f16 v111, v111, v107\n" \
  "ds_write_b128 %[o10], v[108:111] offset:5280\nds_add_u32 %[o13], v146\ns_waitcnt lgkmcnt(3)\nv_cmp_gt_u32 vcc, %[o6], v145\ns_cbranch_vccnz .Lss1%=\n.Lbs1%=:\n" \
  "v_pk_fma_f32 v[124:125], v[124:125], v[136:137], v[40:41]\nv_pk_fma_f32 v[126:127], v[126:127], v[138:139], v[42:43]\nv_pk_fma_f32 v[128:129], v[128:129], v[140:141], v[44:45]\n" \
  "v_pk_fma_f32 v[130:131], v[130:131], v[142:143], v[46:47]\ns_add_u32 %[o6], %[o6], 2\nv_cvt_pk_bf16_f32 v132, v124, v126\nv_cvt_pk_bf16_f32 v133, v128, v130\nv_cvt_pk_bf16_f32 v134, v125, v127\n" \
  "v_cvt_pk_bf16_f32 v135, v129, v131\nglobal_store_dwordx2 %[o14], v[132:133], %[o19]\nglobal_store_dwordx2 %[o14], v[134:135], %[o19] offset:1024\n" HST(13) \
  "" HST(11) HST(12) \
  "ds_read_b32 v145, %[o12] offset:1024\nds_read_b64 v[124:125], %[o11] offset:4096\nds_read_b64 v[126:127], %[o11] offset:13312\nds_read_b64 v[128:129], %[o11] offset:22528\n" \
  "ds_read_b64 v[130:131], %[o11] offset:31744\ns_waitcnt vmcnt(2)\nds_add_u32 %[o13], v146 offset:2048\ns_waitcnt lgkmcnt(1)\nv_cmp_gt_u32 vcc, %[o6], v145\ns_cbranch_vccnz .Lss2%=\n.Lbs2%=:\n" \
  "v_pk_fma_f32 v[124:125], v[124:125], v[136:137], v[48:49]\nv_pk_fma_f32 v[126:127], v[126:127], v[138:139], v[50:51]\nv_pk_fma_f32 v[128:129], v[128:129], v[140:141], v[52:53]\n" \
  "v_pk_fma_f32 v[130:131], v[130:131], v[142:143], v[54:55]\ns_add_u32 %[o6], %[o6], 2\nv_cvt_pk_bf16_f32 v132, v124, v126\nv_cvt_pk_bf16_f32 v133, v128, v130\nv_cvt_pk_bf16_f32 v134, v125, v127\n" \
  "v_cvt_pk_bf16_f32 v135, v129, v131\nglobal_store_dwordx2 %[o14], v[132:133], %[o20]\nglobal_store_dwordx2 %[o14], v[134:135], %[o20] offset:1024\nds_read_b32 v145, %[o12] offset:1024\n" \
  "ds_read_b64 v[124:125], %[o11] offset:6144\nds_read_b64 v[126:127], %[o11] offset:15360\nds_read_b64 v[128:129], %[o11] offset:24576\nds_read_b64 v[130:131], %[o11] offset:33792\ns_waitcnt vmcnt(2)\n" \
  "ds_add_u32 %[o13], v146 offset:2048\ns_waitcnt lgkmcnt(1)\nv_cmp_gt_u32 vcc, %[o6], v145\ns_cbranch_vccnz .Lss3%=\n.Lbs3%=:\nv_pk_fma_f32 v[124:125], v[124:125], v[136:137], v[56:57]\n" \
  "v_pk_fma_f32 v[126:127], v[126:127], v[138:139], v[58:59]\nv_pk_fma_f32 v[128:129], v[128:129], v[140:141], v[60:61]\nv_pk_fma_f32 v[130:131], v[130:131], v[142:143], v[62:63]\n" \
  "s_add_u32 %[o6], %[o6], 2\nv_cvt_pk_bf16_f32 v132, v124, v126\nv_cvt_pk_bf16_f32 v133, v128, v130\nv_cvt_pk_bf16_f32 v134, v125, v127\nv_cvt_pk_bf16_f32 v135, v129, v131\n" \
  "global_store_dwordx2 %[o14], v[132:133], %[o21]\nglobal_store_dwordx2 %[o14], v[134:135], %[o21] offset:1024\n" HST(6) \
  "s_waitcnt vmcnt(2)\nds_add_u32 %[o13], v146 offset:2048\ns_waitcnt vmcnt(0)\nds_add_u32 %[o13], v146 offset:2048\ns_branch .Le%=\n.Lsp0%=:\ns_lshr_b32 %[o4], %[o22], 4\n.Llp0%=:\n" \
  "s_movk_i32 vcc_lo, 0\nbuffer_load_dwordx4 v[64:67], %[o15], %[o17], vcc_lo offen sc1\ns_movk_i32 vcc_lo, 4096\nbuffer_load_dwordx4 v[68:71], %[o15], %[o17], vcc_lo offen sc1\ns_movk_i32 vcc_lo, 8192\n" \
  "buffer_load_dwordx4 v[72:75], %[o15], %[o17], vcc_lo offen sc1\ns_movk_i32 vcc_lo, 12288\nbuffer_load_dwordx4 v[76:79], %[o15], %[o17], vcc_lo offen sc1\ns_waitcnt vmcnt(0)\n" \
  "s_sub_u32 %[o4], %[o4], 1\ns_cbranch_scc1 .Lbp0%=\nv_cmp_ne_u32 vcc, %[o16], v65\nv_cmp_ne_u32 %[o5], %[o16], v67\ns_or_b64 vcc, vcc, %[o5]\ns_cbranch_scc1 .Llp0%=\ns_branch .Lbp0%=\n.Lsp1%=:\n" \
  "s_lshr_b32 %[o4], %[o22], 4\n.Llp1%=:\ns_movk_i32 vcc_lo, 4096\nbuffer_load_dwordx4 v[68:71], %[o15], %[o17], vcc_lo offen sc1\ns_movk_i32 vcc_lo, 8192\n" \
  "buffer_load_dwordx4 v[72:75], %[o15], %[o17], vcc_lo offen sc1\ns_movk_i32 vcc_lo, 12288\nbuffer_load_dwordx4 v[76:79], %[o15], %[o17], vcc_lo offen sc1\ns_waitcnt vmcnt(0)\n" \
  "s_sub_u32 %[o4], %[o4], 1\ns_cbranch_scc1 .Lbp1%=\nv_cmp_ne_u32 vcc, %[o16], v69\nv_cmp_ne_u32 %[o5], %[o16], v71\ns_or_b64 vcc, vcc, %[o5]\ns_cbranch_scc1 .Llp1%=\ns_branch .Lbp1%=\n.Lsp2%=:\n" \
  "s_lshr_b32 %[o4], %[o22], 4\n.Llp2%=:\ns_movk_i32 vcc_lo, 8192\nbuffer_load_dwordx4 v[72:75], %[o15], %[o17], vcc_lo offen sc1\ns_movk_i32 vcc_lo, 12288\n" \
  "buffer_load_dwordx4 v[76:79], %[o15], %[o17], vcc_lo offen sc1\ns_waitcnt vmcnt(0)\ns_sub_u32 %[o4], %[o4], 1\ns_cbranch_scc1 .Lbp2%=\nv_cmp_ne_u32 vcc, %[o16], v73\nv_cmp_ne_u32 %[o5], %[o16], v75\n" \
  "s_or_b64 vcc, vcc, %[o5]\ns_cbranch_scc1 .Llp2%=\ns_branch .Lbp2%=\n.Lsw0%=:\ns_mov_b32 %[o4], %[o22]\n.Llw0%=:\nds_read_b32 v144, %[o12]\ns_sub_u32 %[o4], %[o4], 1\ns_waitcnt lgkmcnt(0)\n" \
  "v_cmp_gt_u32 vcc, 2, v144\ns_cbranch_scc1 .Low0%=\ns_cbranch_vccnz .Llw0%=\n.Low0%=:\nds_read_b128 v[80:83], %[o9] offset:0\nds_read_b128 v[84:87], %[o9] offset:16\ns_waitcnt lgkmcnt(0)\n" \
  "s_branch .Lbw0%=\n.Lsw1%=:\ns_mov_b32 %[o4], %[o22]\n.Llw1%=:\nds_read_b32 v144, %[o12]\ns_sub_u32 %[o4], %[o4], 1\ns_waitcnt lgkmcnt(0)\nv_cmp_gt_u32 vcc, 3, v144\ns_cbranch_scc1 .Low1%=\n" \
  "s_cbranch_vccnz .Llw1%=\n.Low1%=:\nds_read_b128 v[88:91], %[o9] offset:1024\nds_read_b128 v[92:95], %[o9] offset:1040\ns_waitcnt lgkmcnt(0)\ns_branch .Lbw1%=\n.Lsp3%=:\ns_lshr_b32 %[o4], %[o22], 4\n" \
  ".Llp3%=:\ns_movk_i32 vcc_lo, 12288\nbuffer_load_dwordx4 v[76:79], %[o15], %[o17], vcc_lo offen sc1\ns_waitcnt vmcnt(0)\ns_sub_u32 %[o4], %[o4], 1\ns_cbranch_scc1 .Lbp3%=\n" \
  "v_cmp_ne_u32 vcc, %[o16], v77\nv_cmp_ne_u32 %[o5], %[o16], v79\ns_or_b64 vcc, vcc, %[o5]\ns_cbranch_scc1 .Llp3%=\ns_branch .Lbp3%=\n.Lss0%=:\ns_mov_b32 %[o4], %[o22]\n.Lls0%=:\n" \
  "ds_read_b32 v145, %[o12] offset:1024\ns_sub_u32 %[o4], %[o4], 1\ns_waitcnt lgkmcnt(0)\nv_cmp_gt_u32 vcc, %[o6], v145\ns_cbranch_scc1 .Los0%=\ns_cbranch_vccnz .Lls0%=\n.Los0%=:\n" \
  "ds_read_b64 v[124:125], %[o11] offset:0\nds_read_b64 v[126:127], %[o11] offset:9216\nds_read_b64 v[128:129], %[o11] offset:18432\nds_read_b64 v[130:131], %[o11] offset:27648\ns_waitcnt lgkmcnt(0)\n" \
  "s_branch .Lbs0%=\n.Lsw2%=:\ns_mov_b32 %[o4], %[o22]\n.Llw2%=:\nds_read_b32 v144, %[o12]\ns_sub_u32 %[o4], %[o4], 1\ns_waitcnt lgkmcnt(0)\nv_cmp_gt_u32 vcc, 4, v144\ns_cbranch_scc1 .Low2%=\n" \
  "s_cbranch_vccnz .Llw2%=\n.Low2%=:\nds_read_b128 v[80:83], %[o9] offset:2048\nds_read_b128 v[84:87], %[o9] offset:2064\ns_waitcnt lgkmcnt(0)\ns_branch .Lbw2%=\n.Lss1%=:\ns_mov_b32 %[o4], %[o22]\n" \
  ".Lls1%=:\nds_read_b32 v145, %[o12] offset:1024\ns_sub_u32 %[o4], %[o4], 1\ns_waitcnt lgkmcnt(0)\nv_cmp_gt_u32 vcc, %[o6], v145\ns_cbranch_scc1 .Los1%=\ns_cbranch_vccnz .Lls1%=\n.Los1%=:\n" \
  "ds_read_b64 v[124:125], %[o11] offset:2048\nds_read_b64 v[126:127], %[o11] offset:11264\nds_read_b64 v[128:129], %[o11] offset:20480\nds_read_b64 v[130:131], %[o11] offset:29696\n" \
  "s_waitcnt lgkmcnt(0)\ns_branch .Lbs1%=\n.Lss2%=:\ns_mov_b32 %[o4], %[o22]\n.Lls2%=:\nds_read_b32 v145, %[o12] offset:1024\ns_sub_u32 %[o4], %[o4], 1\ns_waitcnt lgkmcnt(0)\n" \
  "v_cmp_gt_u32 vcc, %[o6], v145\ns_cbranch_scc1 .Los2%=\ns_cbranch_vccnz .Lls2%=\n.Los2%=:\nds_read_b64 v[124:125], %[o11] offset:4096\nds_read_b64 v[126:127], %[o11] offset:13312\n" \
  "ds_read_b64 v[128:129], %[o11] offset:22528\nds_read_b64 v[130:131], %[o11] offset:31744\ns_waitcnt lgkmcnt(0)\ns_branch .Lbs2%=\n.Lss3%=:\ns_mov_b32 %[o4], %[o22]\n.Lls3%=:\n" \
  "ds_read_b32 v145, %[o12] offset:1024\nds_read_b64 v[124:125], %[o11] offset:6144\nds_read_b64 v[126:127], %[o11] offset:15360\nds_read_b64 v[128:129], %[o11] offset:24576\n" \
  "ds_read_b64 v[130:131], %[o11] offset:33792\ns_sub_u32 %[o4], %[o4], 1\ns_waitcnt lgkmcnt(0)\nv_cmp_gt_u32 vcc, %[o6], v145\ns_cbranch_scc1 .Lbs3%=\ns_cbranch_vccnz .Lls3%=\ns_branch .Lbs3%=\n.Le%=:\n"
#define STR_(x) #x
#define STR(x) STR_(x)
#define HPRIO "s_setprio " STR(PH) "\n"

typedef u64 u64x2 __attribute__((ext_vector_type(2)));
typedef u64 u64x4 __attribute__((ext_vector_type(4)));
typedef u64 u64x8 __attribute__((ext_vector_type(8)));
typedef const GAS u16* gp;
#define KSW
#define AUXF 7

__global__ __launch_bounds__(512, 2) __attribute__((amdgpu_num_vgpr(127))) void fused_kernel(
    const u16* a0, const u16* a1, const u16* a2, const u16* a3, const u16* a4, const u16* a5, const u16* a6, const u16* a7,
    const u16* a8, const u16* a9, const u16* a10, const u16* a11, const u16* a12, u16* a13, char* a14) {
  __shared__ __attribute__((aligned(16))) char smem[LDS_BYTES];
  LAS char* sm = (LAS char*)smem;
  STAMP_DECL;
  STAMP(0);
  u64x8 ka; u64x4 kb4; u64x2 kc; u64 kd;
  asm volatile("s_load_dwordx16 %0, %4, 0\n\ts_load_dwordx8 %1, %4, 0x40\n\ts_load_dwordx4 %2, %4, 0x60\n\ts_load_dwordx2 %3, %4, 0x70" KSW
               : "=&s"(ka), "=&s"(kb4), "=&s"(kc), "=&s"(kd) : "s"(__builtin_amdgcn_kernarg_segment_ptr()));
  SB();
#define RSRC() const __amdgpu_buffer_rsrc_t wr = __builtin_amdgcn_make_buffer_rsrc((void*)ws, 0, WS_BYTES, 0x00020000)
  const char* pc0 = (const char*)__builtin_amdgcn_s_getpc();
  const int tid = threadIdx.x, lane = tid & 63;
  const int w = __builtin_amdgcn_readfirstlane(tid >> 6);
  const int c = lane & 15, g = lane >> 4, q = (lane >> 2) & 3, p = lane & 3;
  const int b = blockIdx.x, xs = b & 7, idx = b >> 3;
  const f32x4 z4 = {0.f, 0.f, 0.f, 0.f};
  const u32x4 zz = {0u, 0u, 0u, 0u};
  asm volatile("s_cmp_lt_u32 %0, 6\n\ts_cbranch_scc1 1f\n\tglobal_load_dword v255, %1, %2\n1:"
               :: "s"(w), "v"(min((tid & 127) * 128, CODE_MAX)), "s"(pc0) : "scc", "memory");

  asm volatile("s_cmp_gt_u32 %4, 63\n\ts_cbranch_scc1 1f\n\tv_lshl_add_u32 v120, %4, 15, %0\n\ts_movk_i32 s97, 0x300\n\tv_add_u32 v121, 0x100, v120\n\t"
               "v_bfi_b32 v121, s97, v121, v120\n\tv_xor_b32 v122, 0x200, v120\n\tv_xor_b32 v123, 0x200, v121\n\ts_waitcnt lgkmcnt(0)\n\t"
               "global_load_dwordx4 v[116:119], %1, %3\n\tglobal_load_dwordx4 v[100:103], v120, %2 nt\n\tglobal_load_dwordx4 v[104:107], v121, %2 nt\n\t"
               "global_load_dwordx4 v[108:111], v122, %2 nt\n\tglobal_load_dwordx4 v[112:115], v123, %2 nt\n\t"
               "s_load_dword s96, %8, 0\n1:"
               :: "v"((4 * w + q) * 1024 + 256 * q + 64 * g + 16 * p), "v"(256 * q + 64 * g + 16 * p), "s"(ka[0]), "s"(kb4[1]), "s"(b), "s"(ka), "s"(kb4), "s"(kc), "s"(kd)
               : "scc", "memory", "s96", "s97", "v100", "v101", "v102", "v103", "v104", "v105", "v106", "v107", "v108", "v109", "v110", "v111", "v112", "v113", "v114", "v115", "v116", "v117", "v118", "v119", "v120", "v121", "v122", "v123");
  if (EXP1(idx < 16)) {
    int tid = threadIdx.x;
    PIN(tid);
    const int lane = tid & 63, c = lane & 15, g = lane >> 4, q = (lane >> 2) & 3, p = lane & 3;
    const u32x4 ones = {0x3f803f80u, 0x3f803f80u, 0x3f803f80u, 0x3f803f80u};
    GAS char* ws;
    u32 tag, tag0;
    u32x4 xv[4];
#define GOTTAG() asm volatile("s_waitcnt lgkmcnt(0)\n\ts_add_u32 %0, %1, 1" : "=s"(tag) : "s"(tag0) : "scc", "memory")
    if (EXP2(idx < 8)) {
      const int un = 8 * b + w;
      asm volatile("s_movk_i32 s97, 0x5757" ::: "s97");
      asm volatile("s_cmp_lt_u32 %3, 4\n\ts_cbranch_scc1 1f\n\tglobal_load_dword v255, %1, %2\n1:"
                   :: "n"(0), "v"((xs * 8 + idx) * 32768 + (tid & 255) * 128), "s"(kc[0]), "s"(xs == AUXF ? 0 : w) : "scc", "memory");
      u32x4 wv[4];
      asm volatile("s_mov_b64 %0, %1 ; late" : "=&s"(ws) : "s"(kd) : "memory");
      STAMP(11);
      RSRC();
      f32x4 d0 = z4, d1 = z4, d2 = z4, d3 = z4;
      u32x4 on = ones;
      PIN(on);
      const bool lead = lane == 20 * g;
      u32 sto = lead ? WS_STATS + un * 32 + g * 8 : ~15u, kid = 2048 - (4 * un + g), kso = lead ? w * 32 + g * 8 : 256 + lane * 8;
      PIN(sto); PIN(kid); PIN(kso);
      asm volatile("s_cmp_lt_u32 %1, 4\n\ts_waitcnt vmcnt(5)\n\ts_cbranch_scc0 1f\n\ts_waitcnt vmcnt(4)\n1:" : "={v[116:119]}"(wv[0]) : "s"(!SPF || xs == AUXF ? 0 : w) : "scc");
#pragma unroll
      for (int k = 0; k < 4; ++k) { wv[1][k] = dppu<ROR(12)>(wv[0][k]); wv[2][k] = dppu<ROR(8)>(wv[0][k]); wv[3][k] = dppu<ROR(4)>(wv[0][k]); }
#pragma unroll
      for (int j = 0; j < 4; ++j) d3 = mma_b(on, wv[j], d3);
      float sw = trace4(d3);
      PIN(sw); PIN(wv[1]); PIN(wv[2]); PIN(wv[3]);
      asm volatile("s_waitcnt lgkmcnt(0)\n\ts_add_u32 %0, s96, 1" : "=s"(tag) :: "scc", "memory");
      u32x2 gs = {0u, tag};
      PIN(gs.y);
      SB();
      asm volatile("s_cmp_lt_u32 %4, 4\n\ts_waitcnt vmcnt(1)\n\ts_cbranch_scc0 1f\n\ts_waitcnt vmcnt(0)\n1:"
                   : "={v[100:103]}"(xv[0]), "={v[104:107]}"(xv[1]), "={v[108:111]}"(xv[2]), "={v[112:115]}"(xv[3]) : "s"(!SPF || xs == AUXF ? 0 : w) : "scc");
#pragma unroll
      for (int j = 0; j < 4; ++j) { d0 = mma_b(on, xv[j], d0); d2 = mma_b(xv[j], xv[j], d2); }
      asm volatile("" : "+v"(d0), "+v"(d2), "+v"(xv[0]));
#pragma unroll
      for (int j = 0; j < 4; ++j) d1 = mma_b(wv[j], xv[j], d1);
      asm volatile("" : "+v"(d0), "+v"(d2), "+v"(d1));
      STAMP(1);
      const float mean = trace4(d0) * (1.f / 512.f);
      const float var = fmaxf(__builtin_fmaf(-mean, mean, trace4(d2) * (1.f / 512.f)), 0.f);
      const float sd = __builtin_amdgcn_sqrtf(rbf(rbf(var) + 1e-5f));
      const u32 ms = pkbf(mean, sd);
      gs.x = ms;
      const float r = __builtin_fmaf(-bflo(ms), sw, trace4(d1)) * __builtin_amdgcn_rcpf(bfhi(ms));
      u32 rb = pkbf(r, r) & 0xffff0000u;
      rb ^= ((int)rb < 0) ? 0xffff0000u : 0x80000000u;
      u32x2 km = {rb | kid, ms};
      lds_st<u32x2>(sm, kso, km);
      bar();
      STAMP(2);
      if (w == 0) {
        km = lds_ld<u32x2>(sm, (lane & 31) * 8);
        const u32 k1 = max16(rowmax(km.x));
        const u32 k2 = max16(rowmax(km.x == k1 ? 0u : km.x));
        u32x4 gk = {km.x, tag, km.y, tag};
        if (lane < 32 && km.x >= k2) __builtin_amdgcn_raw_buffer_store_b128(gk, wr, WS_KEYS + b * 32 + (km.x == k1 ? 0 : 16), 0, 16);
        STAMP(7);
      }
      if (w == 1) {
        km = lds_ld<u32x2>(sm, (lane & 31) * 8);
        gs.x = km.y;
        if (lane < 32) __builtin_amdgcn_raw_buffer_store_b64(gs, wr, WS_STATS + b * 256 + lane * 8, 0, 16);
      }
      STAMP_END(15);
      asm volatile("s_waitcnt vmcnt(0)");
      __builtin_amdgcn_endpgm();
    }
    if (xs == AUXF) __builtin_amdgcn_endpgm();
    const int fi = xs * 8 + idx - 8;
    const int wq = w & 3;
    gp x;
    const GAS char *pW, *pWd;
    u32 go = (128 * wq + (lane >> 2)) * 3584 + 64 * fi + 16 * p, dl = (32 * fi + (lane >> 3)) * 1024 + 128 * w + 16 * (lane & 7);
    u32 fo = (128 * wq + 64 * (fi & 1) + lane) * 3584 + 64 * fi;
    PIN(go); PIN(dl); PIN(fo);
    if (w == 7) __builtin_amdgcn_s_setprio(3);
    asm volatile("s_waitcnt lgkmcnt(0)\n\ts_load_dword %4, %9, 0\n\ts_cmp_lt_u32 %10, 4\n\ts_mov_b64 %0, %5\n\ts_cselect_b64 %1, %6, %7\n\ts_mov_b64 %2, %8\n\ts_mov_b64 %3, %9"
                 : "=&s"(x), "=&s"(pW), "=&s"(pWd), "=&s"(ws), "=&s"(tag0)
                 : "s"(ka[0]), "s"(kb4[2]), "s"(kb4[3]), "s"(kc[0]), "s"(kd), "s"(w), "s"(ka), "s"(kb4), "s"(kc) : "scc", "memory");
    STAMP(11);
    asm volatile("global_load_dword v255, %1, %2" FPW :: "n"(0), "v"(fo), "s"(pW) : "memory");
#pragma unroll
    for (int i = 0; i < 8; ++i) {
      const int ii = (i & 3) + ((i >> 2) ^ (SWP & fi & 1)) * 4;
      __builtin_amdgcn_global_load_lds((const GAS void*)(pW + ii * 16 * 3584 + go), sm + AX_GU + w * 8192 + ii * 1024, 16, 0, WAUX);
    }
#pragma unroll
    for (int i = 0; i < 4; ++i) __builtin_amdgcn_global_load_lds((const GAS void*)(pWd + i * 8192 + dl), sm + AX_WD + w * 4096 + i * 1024, 16, 0, DAUX);
    STAMP(8);
    RSRC();
    u32 ko = WS_KEYS + lane * 16;
    u32x4 kk[2];
#define KLOAD() asm volatile("buffer_load_dwordx4 %0, %2, %3, 0 offen sc1\n\tbuffer_load_dwordx4 %1, %2, %3, 0 offen offset:1024 sc1" \
                             : "=&v"(kk[0]), "=&v"(kk[1]) : "v"(ko), "s"(wr) : "memory")
#define KWAIT(n) asm volatile("s_waitcnt vmcnt(" #n ")" : "+v"(kk[0]), "+v"(kk[1]) :: "memory")
    u32 xo = AX_X + (q & 1) * 1088 + 256 * wq + 16 * g, xo2 = AX_X + (q & 1) * 1088 + 128 * w + 32 * p + 8 * g, io = AX_I + 8 * (q & 1);
    u32 so = p ? 2048 + lane * 16 : (((w >> 2) * 2 + (q & 1)) * 4 + wq) * 128 + (q >> 1) * 64 + 16 * g, ro = q * 512 + 32 * g + 8 * p;
    u32 po = WS_PART + ((16 * w + 4 * p + g) * NPROD + fi) * 32 + q * 16;
    if (c >= 8) po = ~15u;
    u32 m0 = p == 0 ? ~0u : 0u, m1 = p == 1 ? ~0u : 0u, m2 = p == 2 ? ~0u : 0u, m3 = p == 3 ? ~0u : 0u;
    u32 o = AX_GU + w * 8192 + (8 * g + q) * 64 + 8 * p, o2 = AX_WD + w * 4096 + (8 * g + q) * 128 + 8 * p;
    PIN(ko); PIN(xo); PIN(xo2); PIN(io); PIN(so); PIN(ro); PIN(po); PIN(m0); PIN(m1); PIN(m2); PIN(m3); PIN(o); PIN(o2);
    GOTTAG();
    if (w == 7) { KLOAD(); asm volatile("s_waitcnt vmcnt(6)" ::: "memory"); }
    else asm volatile("s_waitcnt vmcnt(4)" ::: "memory");
    STAMP(10);
    u32x4 fa[8], fb[4];
    f32x4 dsa = z4, dsb = z4;
#define TR8(t, a, s0, s1) asm volatile(                                                                                                                  \
      "ds_read_b64_tr_b16 %0, %8\n\tds_read_b64_tr_b16 %1, %8 offset:%9\n\tds_read_b64_tr_b16 %2, %8 offset:%10\n\tds_read_b64_tr_b16 %3, %8 offset:%9+%10\n\t" \
      "ds_read_b64_tr_b16 %4, %8 offset:2*%10\n\tds_read_b64_tr_b16 %5, %8 offset:%9+2*%10\n\tds_read_b64_tr_b16 %6, %8 offset:3*%10\n\t"               \
      "ds_read_b64_tr_b16 %7, %8 offset:%9+3*%10\n\ts_waitcnt lgkmcnt(0)"                                                                               \
      : "=&v"(t[0]), "=&v"(t[1]), "=&v"(t[2]), "=&v"(t[3]), "=&v"(t[4]), "=&v"(t[5]), "=&v"(t[6]), "=&v"(t[7]) : "v"(a), "n"(s0), "n"(s1) : "memory")
    {
      u32x2 t[8], u[8], v[8];
      TR8(t, o, 256, 2048);
#pragma unroll
      for (int j = 0; j < 4; ++j) { fa[j] = u32x4{t[2 * j].x, t[2 * j].y, t[2 * j + 1].x, t[2 * j + 1].y}; dsa = mma_b(fa[j], ones, dsa); }
      TR8(u, o + 32, 256, 2048);
#pragma unroll
      for (int j = 0; j < 4; ++j) { fa[4 + j] = u32x4{u[2 * j].x, u[2 * j].y, u[2 * j + 1].x, u[2 * j + 1].y}; dsb = mma_b(fa[4 + j], ones, dsb); }
      if (w == 7) asm volatile("s_waitcnt vmcnt(2)" ::: "memory");
      else asm volatile("s_waitcnt vmcnt(0)" ::: "memory");
      TR8(v, o2, 512, 32);
#pragma unroll
      for (int j = 0; j < 4; ++j) { fb[j] = u32x4{v[2 * j].x, v[2 * j].y, v[2 * j + 1].x, v[2 * j + 1].y}; PIN(fb[j]); }
    }
    if (c & 8) dsa = dsb;
    PIN(dsa);
    STAMP(9);
    if (w == 7) {
      KWAIT(0);
#pragma unroll 1
      for (int spin = 0; spin < SPIN_CAP; ++spin) {
        if (__all((kk[0].y == tag) & (kk[0].w == tag) & (kk[1].y == tag) & (kk[1].w == tag))) break;
        KLOAD();
        KWAIT(0);
      }
      STAMP(4);
      const u32 K1 = wavemax(max(kk[0].x, kk[1].x));
      __builtin_amdgcn_global_load_lds((const GAS void*)(x + ((2048 - K1) & 2047) * 512 + 8 * lane), sm + AX_X, 16, 0, 0);
      const u32 K2 = wavemax(max(kk[0].x == K1 ? 0u : kk[0].x, kk[1].x == K1 ? 0u : kk[1].x));
      __builtin_amdgcn_global_load_lds((const GAS void*)(x + ((2048 - K2) & 2047) * 512 + 8 * lane), sm + AX_X + 1088, 16, 0, 0);
#pragma unroll
      for (int j = 0; j < 2; ++j)
        asm volatile("ds_write_b64 %0, %1" :: "v"(kk[j].x == K1 ? AX_I : kk[j].x == K2 ? AX_I + 8 : AX_I + 16), "v"(u32x2{kk[j].x, kk[j].z}) : "memory");
      asm volatile("s_waitcnt vmcnt(0)" ::: "memory");
    }
    bar();
    STAMP(3);
#pragma unroll
    for (int j = 0; j < 4; ++j) xv[j] = lds_ld<u32x4>(sm, xo + 64 * j);
    const u32x2 inf = lds_ld<u32x2>(sm, io);
    const u32x2 xres = lds_ld<u32x2>(sm, xo2);
    SB();
    f32x4 d = z4, e = z4;
#pragma unroll
    for (int j = 0; j < 4; ++j) { d = mma_b(fa[j], xv[j], d); e = mma_b(fa[4 + j], xv[j], e); }
    const u32 key = inf.x, ms = inf.y;
    const u32 row = (2048 - key) & 2047;
    u32 rb = key & 0xffff0000u;
    rb ^= ((int)rb < 0) ? 0x80000000u : 0xffff0000u;
    float sg = rbf(__builtin_amdgcn_rcpf(1.f + __builtin_amdgcn_exp2f(__uint_as_float(rb) * -1.44269504f)));
    float rs = __builtin_amdgcn_rcpf(bfhi(ms));
    f32x4 ci = {bflo(xres.x), bfhi(xres.x), bflo(xres.y), bfhi(xres.y)};
    if (fi != 0) ci = z4;
    PIN(sg); PIN(rs); PIN(ci);
    if (c & 8) d = e;
    d += dsa * -bflo(ms);
    lds_st<f32x4>(sm, so, d);
    bar();
    STAMP(5);
    {
      const f32x2 s0 = lds_ld<f32x2>(sm, ro), s1 = lds_ld<f32x2>(sm, ro + 128), s2 = lds_ld<f32x2>(sm, ro + 256), s3 = lds_ld<f32x2>(sm, ro + 384);
      const u32 vv = pkbf(((s0.x + s1.x) + (s2.x + s3.x)) * rs, ((s0.y + s1.y) + (s2.y + s3.y)) * rs);
      const u32 ov = dppu<ROR(8)>(vv);
      const float ga = (c & 8) ? bfhi(ov) : bflo(vv), up = (c & 8) ? bfhi(vv) : bflo(ov);
      const float sl = ga * __builtin_amdgcn_rcpf(1.f + __builtin_amdgcn_exp2f(ga * -1.44269504f));
      const float hv = rbf(sl) * (up * sg);
      const u32 hp = pkbf(hv, dppf<ROR(8)>(hv));
      const u32x4 h = {dppu<0x00>(hp), dppu<0x55>(hp), dppu<0xaa>(hp), dppu<0xff>(hp)};
      const f32x4 y = mma_b(fb[3], h & m3, mma_b(fb[2], h & m2, mma_b(fb[1], h & m1, mma_b(fb[0], h & m0, ci))));
      const u32 tw = tag ^ (row << 21);
      u32x4 gp = {pkbf(y.x, y.y), tw, pkbf(y.z, y.w), tw};
      __builtin_amdgcn_raw_buffer_store_b128(gp, wr, po, 0, 16);
    }
    STAMP(6);
    STAMP_END(15);
    __builtin_amdgcn_endpgm();
  }

  const int ch = w & 3, hb = w >> 2;
  u32 vAre, vAim, vBre, vBim, vCre, vCim, sld, sds, tag;
  u32x2 xc[8];
  u32 xpo = ((idx & 15) * 64 + (3 - w) * 1024 + lane) * 1024;
  u32 ppo = ((idx & 15) * 4 + ch) * 256 + lane * 4;
#define COL(i) asm volatile("s_cmp_eq_u32 %4, 0\n\ts_cbranch_scc1 1f\n\t"                                                        \
               "global_load_dwordx2 %0, %5, %6 " CPOL "\n\tglobal_load_dwordx2 %1, %5, %6 offset:1024 " CPOL "\n\t"                              \
               "global_load_dwordx2 %2, %5, %7 " CPOL "\n\tglobal_load_dwordx2 %3, %5, %7 offset:1024 " CPOL "\n1:"                                \
               : "=&v"(xc[i]), "=&v"(xc[i + 1]), "=&v"(xc[i + 2]), "=&v"(xc[i + 3]) : "s"(hb), "v"(go), "s"(x + (i) * 131072), "s"(x + (i) * 131072 + 262144) : "scc", "memory")
  gp x;
  GAS u16* out;
  GAS char* ws;
  u32 col;
  asm volatile("s_waitcnt lgkmcnt(0)\n\ts_lshr_b32 %0, %2, 7\n\ts_sub_u32 %0, %1, %0\n\ts_and_b32 %0, %0, 7" : "=&s"(col) : "s"(xs + 4), "s"((u32)kc[1]), "s"(ka), "s"(kb4), "s"(kc), "s"(kd) : "scc", "memory");
  asm volatile("v_lshl_add_u32 %6, %15, 14, %6\n\t"
               "global_load_dword %0, %6, %9 " PPOL "\n\tglobal_load_dword %1, %6, %10 " PPOL "\n\tglobal_load_dword %2, %6, %11 " PPOL "\n\t"
               "global_load_dword %3, %6, %12 " PPOL "\n\tglobal_load_dword %4, %6, %13 " PPOL "\n\tglobal_load_dword %5, %6, %14 " PPOL "\n\t"
               "v_lshl_add_u32 %7, %15, 7, %7\n\ts_cmp_lg_u32 %16, 1\n\ts_cbranch_scc1 1f\n\tglobal_load_dword v255, %7, %8\n1:"
               : "=&v"(vAre), "=&v"(vAim), "=&v"(vBre), "=&v"(vBim), "=&v"(vCre), "=&v"(vCim), "+&v"(ppo), "+&v"(xpo)
               : "s"(ka[0]), "s"(ka[1]), "s"(ka[2]), "s"(ka[3]), "s"(ka[4]), "s"(ka[5]), "s"(ka[6]), "s"(col), "s"(w >> 1) : "scc", "memory");
  asm volatile("s_load_dword %0, %7, %11\n\ts_load_dword %1, %8, %11\n\ts_load_dword %2, %10, 0\n\ts_mov_b64 %3, %6\n\ts_mov_b64 %4, %9\n\ts_mov_b64 %5, %10"
               : "=&s"(sld), "=&s"(sds), "=&s"(tag), "=&s"(x), "=&s"(out), "=&s"(ws)
               : "s"(ka[0]), "s"(ka[7]), "s"(kb4[0]), "s"(kc[1]), "s"(kd), "s"(col * 128 + (idx & 15) * 8 + (ch & 2) * 2) : "memory");
  STAMP(8);
  RSRC();
  const int grp = col * 16 + (idx & 15);
  const u32 go = (128 * ch + 2 * lane) * 1024 + grp * 8;
#pragma unroll
  for (int i = 0; i < 3; ++i) lds_st<u32x4>(sm, FL_BASE + i * 8192 + tid * 16, zz);
  {
    const int k = tid >> 5, l = tid & 31;
    u32 o = U_BASE + k * UCH + min(l, 29) * 16;
    const u64 wo = (u64)W5_OFF << 16 | (u64)W7_OFF << 32 | (u64)W9_OFF << 48 | W3_OFF;
    u32 o4 = W_BASE - 32 + (l >> 3) * WCH + ((u32)(wo >> ((l & 6) * 8)) & 0xffffu) + (l & 1) * 16;
    PIN(o4);
    o = k == 4 ? o4 : o;
    o = k > 4 ? ST_BASE - 16 - (tid - 160) * 16 : o;
    if (tid < 256) lds_st<u32x4>(sm, o, zz);
  }
  const u32 tb = ch * YCH + hb * TAB_W;
  const u32 kb = KB_BASE + ch * KCH;
  u32 ta = tb + g * 16 + c * TAB_RS;
  u32 t1a = tb + g * 16 + (u32)((0xB766654443222100ull >> (c * 4)) & 15) * TAB_RS;
  u32 sa = ST_BASE + w * ST_W + (g >> 1) * 640 + 16 * c + 8 * (g & 1);
  u32 zs = ST_BASE + 8 * ST_W + ch * 512 + 8 * lane;
  u32 fl = FL_BASE + p * 256;
  u32 flw = FL_BASE + ch * 256 + lane * 4;
  u32 k3 = WS_STATS + (xs & (SREP - 1)) * SCS + 1024 * ch + 16 * lane;
  const u32 usel = hb ? 0x03020c0cu : 0x01000c0cu;
  PIN(ta); PIN(t1a); PIN(sa); PIN(zs); PIN(fl); PIN(flw);
  PIN(k3);
  u32 k0, k1, k2, k4 = 0, k5 = 0, k6 = 0, k7 = 0, k8 = 0, k9 = 0, k10 = 0, k11 = 0, k12 = 0;
  if (hb == 0) {
      k0 = kb + (g < 2 ? Z2_OFF + 120 + 128 * g + 8 * c : Z3_OFF + 60 + 128 * (g - 2) + 4 * c);
      k1 = k0 + (g < 2 ? 4 : 64);
      k2 = ST_BASE + ch * ST_W + (lane & 1) * 640 + 16 * (lane >> 1);
      k3 = kb + 32 + 16 * lane;
      const int e = min(lane >> 1, 6), cp = lane & 1;
      const int dd = e < 4 ? 256 + (e & 1) : e == 4 ? 277 : 278;
      const bool odd = e == 2 || e == 3 || e == 6;
      k4 = kb + (2 * cp + (odd ? 1 : 0)) * KL_CS + 4 * dd + (odd ? 36 : cp ? 40 : 32) - 4 * cp;
      k5 = ((e == 1 || e == 5) ? 0x0c0c0302u : e == 4 ? 0x03020c0cu : 0x03020302u) | (e < 4 ? 0u : 0x04040404u);
      k6 = kb + 8 * p + ((-q) & 3) * KL_CS + 56 - 16 * g;
      k7 = kb + Z2_OFF + 8 * c + 16 * g;
      k8 = kb + Z3_OFF + 4 * c + 16 * g;
      k9 = U_BASE + ch * UCH + 32 * c + 16 * g;
      k10 = W_BASE + ch * WCH + (q & 1 ? W5_OFF : W3_OFF) + 2 * (384 - 276 + 4) - 8 * (8 * g + 4 + q) + 8 * p;
      k11 = W_BASE + ch * WCH + (q & 1 ? W9_OFF : W7_OFF) + 2 * (1024 - 792 + 4) - 16 * (8 * g + 4 + q) + 8 * p;
      k12 = YO_BASE + ch * YCH + 64 * c + 16 * (g ^ ((c >> 1) & 3));
    PIN(k0); PIN(k1); PIN(k2); PIN(k3); PIN(k4); PIN(k5); PIN(k6); PIN(k7); PIN(k8); PIN(k9); PIN(k10); PIN(k11); PIN(k12);
  } else {
    k0 = U_BASE + ch * UCH + 464 + lane * 16; k1 = W_BASE + ch * WCH + lane * 16; k2 = YO_BASE + 512 * ch + ((lane * 8) ^ (lane & 48));
    PIN(k0); PIN(k1); PIN(k2);
    asm volatile("" : "=v"(k4), "=v"(k5), "=v"(k6), "=v"(k7), "=v"(k8), "=v"(k9), "=v"(k10), "=v"(k11), "=v"(k12));
  }
  asm volatile("s_waitcnt lgkmcnt(0)\n\ts_barrier\n\ts_add_u32 %2, %2, 1" : "+s"(sld), "+s"(sds), "+s"(tag) :: "scc", "memory");
  const float dt = rbf(__builtin_amdgcn_exp2f(__uint_as_float(ch & 1 ? sld & 0xffff0000u : sld << 16) * 1.44269504f));
  const f32x2 cst = {1.44269504f, 0.15915494f};
  f32x2 dc = cst * dt;
  float d1 = 1.f + __uint_as_float(ch & 1 ? sds & 0xffff0000u : sds << 16);
  const int be0 = (int)((__builtin_amdgcn_readfirstlane(__float_as_uint(d1)) >> 23) & 255) - 6;
  const u32 dtb = __builtin_amdgcn_readfirstlane(__float_as_uint(dt)) >> 16;
  if (tid >= 256 || lane) d1 = 0.f;
  PIN(dc); PIN(d1);
#define PWAIT_ "s_mov_b32 m0, %6\n\ts_cmp_eq_u32 %7, 1\n\ts_waitcnt vmcnt(1)\n\ts_cbranch_scc1 1f\n\ts_waitcnt vmcnt(0)\n1:"
  asm volatile(PWAIT_
               : "+v"(vAre), "+v"(vAim), "+v"(vBre), "+v"(vBim), "+v"(vCre), "+v"(vCim) : "s"(tb), "s"(w >> 1) : "scc", "memory");
  u32x4 st[4];
  u32x2 za;
  {
#define UP(v) __uint_as_float(__builtin_amdgcn_perm(v, v, usel))
#define RD(F, a) _Pragma("unroll") for (int k = 0; k < 4; ++k) F[k] = lds_ld<u32x4>(sm, a + k * 64)
#define LATE(i) asm volatile("s_cmp_eq_u32 %1, 0\n\ts_cbranch_scc1 1f\n\tglobal_load_dwordx2 %0, %2, %3 offset:%4 " CPOL "\n1:"              \
                             : "=&v"(xc[i]) : "s"(hb), "v"(go), "s"(x + ((i) >> 1) * 262144), "n"(((i) & 1) * 1024) : "scc", "memory")
    COL(0);
    if (hb) PRIO(PK);
    const f32x2 av = {UP(vAre), UP(vAim)};
    const f32x2 lg = av * dc;
    STAMP(9);
    const f32x2 E16 = seed(lg * 16.f), E1 = seed(lg);
    f32x2 E32, E64, E128, E2, E4, E8;
    asm(CM2("%0", "%6", "%6", "%3", "%7", "%7") CM2("%1", "%0", "%0", "%4", "%3", "%3") CM2("%2", "%1", "%1", "%5", "%4", "%4")
        : "=&v"(E32), "=&v"(E64), "=&v"(E128), "=&v"(E2), "=&v"(E4), "=&v"(E8) : "v"(E16), "v"(E1));
    u32 q1, r1, r2, r3, r4, r5, r6, r7, r8, t0, t1;
    asm volatile("v_cvt_pk_f16_f32 %0, -%10, %11\n\tv_cvt_pk_f16_f32 %1, -%12, %13\n\tv_cvt_pk_f16_f32 %3, -%14, %15\n\t"
                 "v_cvt_pk_f16_f32 %7, -%16, %17\n\t"
                 WT("%18", 0) WT("%0", 1) HM("%2", "%0", "%1") WT("%1", 2) HM("%4", "%0", "%3") WT("%2", 3) HM("%5", "%1", "%3") WT("%3", 4)
                 HM("%6", "%2", "%3") WT("%4", 5) HM("%8", "%0", "%7") WT("%5", 6) HM("%9", "%1", "%7") WT("%6", 7) WT("%8", 9)
                 HM("%8", "%2", "%7") WT("%9", 10) HM("%9", "%3", "%7") WT("%8", 11) HM("%8", "%4", "%7") WT("%9", 12)
                 HM("%9", "%5", "%7") WT("%8", 13) HM("%8", "%6", "%7") WT("%9", 14) WT("%7", 8) WT("%8", 15)
                 : "=&v"(q1), "=&v"(r2), "=&v"(r3), "=&v"(r4), "=&v"(r5), "=&v"(r6), "=&v"(r7), "=&v"(r8), "=&v"(t0), "=&v"(t1)
                 : "v"(E16.x), "v"(E16.y), "v"(E32.x), "v"(E32.y), "v"(E64.x), "v"(E64.y), "v"(E128.x), "v"(E128.y), "v"(0x3c000000u) : "memory");
    u32x4 fq[4], fa[4];
    RD(fq, ta);
    LATE(4);
    us2 mb = __builtin_elementwise_max(__builtin_bit_cast(us2, vBre & 0x7fff7fffu), __builtin_bit_cast(us2, vBim & 0x7fff7fffu));
    us2 mc = __builtin_elementwise_max(__builtin_bit_cast(us2, vCre & 0x7fff7fffu), __builtin_bit_cast(us2, vCim & 0x7fff7fffu));
    mb += mc;
    const float gr = fmaxf(fmaxf(bflo(vAre), __uint_as_float(vAre)), 0.f) * (dc.x * 262400.f);
    const u32 lb = wavemax((u32)max(mb.x, mb.y) + (u32)fminf(gr, 65536.f));
    const int be = max((int)((lb + dtb) >> 7) - 250, be0);
    const int se = min(max(262 - be, 1), 253);
    const float sc = __uint_as_float((u32)se << 23);
    lds_st<u32>(sm, INV_OFF + ch * 8 + (lane < 2 ? 4 * lane : 32), (u32)(254 - se) << 23);
    const f32x2 Bv = f32x2{UP(vBim), UP(vBre)} * dt, Cv = f32x2{UP(vCim), UP(vCre)} * sc;
    f32x2 P0, E256, R0, E512, E516;
    asm(CM2("%0", "%5", "%6", "%1", "%7", "%7") CM2("%2", "%0", "%1", "%3", "%1", "%1") CM("%4", "%3", "%8")
        : "=&v"(P0), "=&v"(E256), "=&v"(R0), "=&v"(E512), "=&v"(E516) : "v"(Cv), "v"(Bv), "v"(E128), "v"(E4));
    LATE(5);
    const u32 z1 = toh(E1), z2 = toh(E2), z4h = toh(E4), z8 = toh(E8);
    asm volatile(WT("%9", 0) HM("%0", "%9", "%10") HM("%1", "%9", "%11") WT("%0", 1) HM("%2", "%0", "%11") WT("%1", 2) HM("%3", "%9", "%12") WT("%2", 3)
                 HM("%4", "%0", "%12") WT("%3", 4) HM("%5", "%1", "%12") WT("%4", 5) HM("%6", "%2", "%12") WT("%5", 6) HM("%7", "%9", "%13") WT("%6", 7)
                 HM("%8", "%0", "%13") WT("%7", 8) HM("%7", "%1", "%13") WT("%8", 9) HM("%8", "%2", "%13") WT("%7", 10) HM("%7", "%3", "%13") WT("%8", 11)
                 HM("%8", "%4", "%13") WT("%7", 12) HM("%7", "%5", "%13") WT("%8", 13) HM("%8", "%6", "%13") WT("%7", 14) WT("%8", 15)
                 : "=&v"(r1), "=&v"(r2), "=&v"(r3), "=&v"(r4), "=&v"(r5), "=&v"(r6), "=&v"(r7), "=&v"(t0), "=&v"(t1)
                 : "v"(toh(P0)), "v"(z1), "v"(z2), "v"(z4h), "v"(z8) : "memory");
    RD(fa, ta);
    LATE(6);
    asm volatile(WT("%5", 0) HM("%0", "%5", "%6") HM("%1", "%5", "%7") WT("%0", 1) HM("%2", "%0", "%7") WT("%1", 2) HM("%3", "%5", "%8") WT("%2", 3)
                 HM("%4", "%0", "%8") WT("%3", 4) HM("%3", "%1", "%8") WT("%4", 5) HM("%4", "%2", "%8") WT("%3", 6)
                 "v_pk_mul_f16 %3, %5, %9 op_sel:[1,0] op_sel_hi:[1,1] neg_lo:[0,1]\n\t"
                 "v_pk_fma_f16 %3, %9, %5, %3 op_sel:[1,0,0] op_sel_hi:[0,0,1]\n\t" WT("%4", 7) WT("%3", 11)
                 : "=&v"(r1), "=&v"(r2), "=&v"(r3), "=&v"(t0), "=&v"(t1)
                 : "v"(toh(R0)), "v"(z2), "v"(z4h), "v"(z8), "v"(q1) : "memory");
    f32x4 a0 = {d1 * sc, 0.f, 0.f, 0.f};
#pragma unroll
    for (int k = 0; k < 4; ++k) a0 = mma_h(fa[k], fq[k], a0);
    RD(fa, t1a);
    LATE(7);
    f32x2 S0, S1, X0, X1, Y0, Y1;
    asm(CM2("%0", "%6", "%8", "%1", "%7", "%8") CM2("%2", "%0", "%8", "%3", "%1", "%8") CM2("%4", "%2", "%9", "%5", "%3", "%9")
        : "=&v"(S0), "=&v"(S1), "=&v"(X0), "=&v"(X1), "=&v"(Y0), "=&v"(Y1) : "v"(P0), "v"(R0), "v"(E516), "v"(E512));
    lds_st<u32x2>(sm, sa, u32x2{pkh(a0.x, a0.y), pkh(a0.z, a0.w)});
    f32x4 a1 = z4;
#pragma unroll
    for (int k = 0; k < 4; ++k) a1 = mma_h(fa[k], fq[k], a1);
    asm volatile(WT("%4", 0) HM("%0", "%4", "%10") WT("%5", 4) HM("%1", "%5", "%10") WT("%0", 1) HM("%2", "%4", "%11") WT("%1", 5) HM("%3", "%5", "%11") WT("%2", 2)
                 HM("%2", "%0", "%11") WT("%3", 6) HM("%3", "%1", "%11") WT("%2", 3) WT("%6", 8) HM("%2", "%6", "%11") WT("%3", 7) WT("%7", 10)
                 HM("%3", "%7", "%11") WT("%2", 9) WT("%8", 12) HM("%2", "%8", "%11") WT("%3", 11) WT("%9", 14) HM("%3", "%9", "%11") WT("%2", 13) WT("%3", 15)
                 : "=&v"(r1), "=&v"(r2), "=&v"(t0), "=&v"(t1)
                 : "v"(toh(S0)), "v"(toh(S1)), "v"(toh(X0)), "v"(toh(X1)), "v"(toh(Y0)), "v"(toh(Y1)), "v"(z4h), "v"(z8) : "memory");
    RD(fa, ta);
    lds_st<u32x2>(sm, sa + 256, u32x2{pkh(a1.x, a1.y), pkh(a1.z, a1.w)});
    STAMP(10);
    f32x4 a2 = z4;
#pragma unroll
    for (int k = 0; k < 4; ++k) a2 = mma_h(fa[k], fq[k], a2);
    za = u32x2{pkh(a2.x, a2.y), pkh(a2.z, a2.w)};
    if (hb) { lds_st<u32x2>(sm, zs, za); lds_vst<u32>(sm, flw, P_ST); }
    asm volatile("s_cmp_eq_u32 %4, 0\n\ts_cbranch_scc1 1f\n\t"
                 "global_load_dwordx4 %0, %5, %6 offset:-4096 sc1\n\tglobal_load_dwordx4 %1, %5, %6 sc1\n\t"
                 "global_load_dwordx4 %2, %5, %7 offset:-4096 sc1\n\tglobal_load_dwordx4 %3, %5, %7 sc1\n1:"
                 : "=&{v[64:67]}"(st[0]), "=&{v[68:71]}"(st[1]), "=&{v[72:75]}"(st[2]), "=&{v[76:79]}"(st[3]) : "s"(hb), "v"(k3), "s"(ws + 4096), "s"(ws + 12288) : "scc", "memory");
  }
  STAMP(1);

  if (hb == 0) {
    const int ln = lane;
    WAITF(0, flw, lds_vld<u32>(sm, flw), "n", P_ST);
    PRIO(PM);
    STAMP(2);
    {
      const u32x4 oa = lds_ld<u32x4>(sm, k2), ob = lds_ld<u32x4>(sm, k2 + 4 * ST_W);
      const u32x2 zb = lds_ld<u32x2>(sm, zs);
      const u32x4 o = {hadd(oa.x, ob.x), hadd(oa.y, ob.y), hadd(oa.z, ob.z), hadd(oa.w, ob.w)};
      lds_st<u32x4>(sm, k3, o);
      const u32 p2 = dppu<0x138>(o.z), p3 = dppu<0x138>(o.w);
      const u32 e0 = __builtin_amdgcn_alignbit(o.x, p3, 16), e1 = __builtin_amdgcn_alignbit(o.y, o.x, 16), e2 = __builtin_amdgcn_alignbit(o.z, o.y, 16),
                e3 = __builtin_amdgcn_alignbit(o.w, o.z, 16), em = __builtin_amdgcn_alignbit(p3, p2, 16);
      lds_st<u32x4>(sm, k3 + 3 * KL_CS, u32x4{e0, e1, e2, e3});
      lds_st<u32x4>(sm, k3 + 2 * KL_CS, u32x4{p3, o.x, o.y, o.z});
      lds_st<u32x4>(sm, k3 + KL_CS, u32x4{em, e0, e1, e2});
      lds_st<u32>(sm, k3 + 2 * KL_CS + 16, o.w);
      lds_st<u32>(sm, k3 + KL_CS + 16, e3);
      const u32 zx = hadd(za.x, zb.x), zy = hadd(za.y, zb.y);
      lds_st<u32>(sm, k0, zx); lds_st<u32>(sm, k1, zy);
      lds_st<u32>(sm, k4, __builtin_amdgcn_perm(__builtin_amdgcn_readlane(zy, 31), __builtin_amdgcn_readlane(o.w, 63), k5));
      asm volatile("" ::: "memory");
    }
    if (tid == 64) __builtin_amdgcn_raw_buffer_store_b32(tag, wr, WS_ACKS + b * 4, 0, 16);
    u32x4 pv;
    u32x2 ak;
    u32 tw, du0;
    u64 du1;
    u32 po = WS_PART + grp * (NPROD * 32) + ln * 16, po3 = po + (ln >= 48 ? 768 : 1024), ao = b == 255 ? WS_ACKS + 512 + ln * 8 : ~15u;
    PIN(po); PIN(po3); PIN(ao);
    STAMP(3);
    {
      u32 cnt;
      asm volatile(MF_ASM
                   : "=&{v[32:35]}"(pv), "=&{v28}"(tw), "=&s"(du0), "=&s"(du1), "=&{v[36:37]}"(ak), "=&s"(cnt)
                   : "v"(k6), "v"(k7), "v"(k8), "v"(k9), "v"(k10), "v"(k11), "v"(k12), "v"(fl), "v"(flw), "v"(po), "v"(po3), "v"(ao), "s"(wr), "s"(w),
                     "n"(SPIN_CAP * 16), "s"(tag)
                   : "vcc", "scc", "memory", MF_CLOB);
    }
    if (w == 0) {
      const u32 t0 = __builtin_amdgcn_readlane(tw, 0), t1 = __builtin_amdgcn_readlane(tw, 1);
      if (lane >= 48) { pv.z = 0u; pv.w = 0u; }
      const float iv = lds_vld<float>(sm, INV_OFF + p * 8);
      STAMP(7);
      const u32 row = (((lane & 32) ? t1 : t0) ^ tag) >> 21;
      const u32 sl = p & 1 ? 0x3f800000u : 0x3f80u;
      const u32x4 sel = {p < 2 ? sl : 0u, p < 2 ? 0u : sl, p < 2 ? sl : 0u, p < 2 ? 0u : sl};
      const f32x4 d = mma_b(pv, sel, z4);
      if (b == 255) {
#pragma unroll 1
        for (int spin = 0; spin < SPIN_CAP; ++spin) {
          if (__all((ak.x == tag) & (ak.y == tag))) break;
          asm volatile("buffer_load_dwordx2 %0, %1, %2, 0 offen sc1\n\ts_waitcnt vmcnt(0)" : "=&v"(ak) : "v"(ao), "s"(wr) : "memory");
        }
        if (lane == 0) __builtin_amdgcn_raw_buffer_store_b32(tag, wr, WS_CTRL, 0, 16);
      }
      const u32 aw = FL_BASE + 2048 + ((row >> 7) & 3) * 256, an = row >> 9;
#pragma unroll 1
      for (int spin = 0; spin < SPIN_CAP * 16; ++spin)
        if (__all(lds_vld<u32>(sm, aw) > an)) break;
      const float yv = lds_vld<float>(sm, YO_BASE + p * YCH + ((row * 4) ^ ((row >> 1) & 48)));
      auto r = __builtin_amdgcn_permlane32_swap(__float_as_uint(d.x + d.z), __float_as_uint(d.y + d.w), false, false);
      const float o = __builtin_fmaf(yv, iv, sum16(__uint_as_float(r[0]) + __uint_as_float(r[1])));
      if ((lane & 31) < 4) out[row * 512 + 4 * grp + p] = (u16)pkbf(o, o);
      STAMP(8);
    }
  } else {
    f32x16 xa, xb;
#define UNP(X, h, i) _Pragma("unroll") for (int e = 0; e < 2; ++e) {                                     \
      X[8 * (h) + e] = bflo(xc[2 * (i) + e].x); X[8 * (h) + 2 + e] = bfhi(xc[2 * (i) + e].x);              \
      X[8 * (h) + 4 + e] = bflo(xc[2 * (i) + e].y); X[8 * (h) + 6 + e] = bfhi(xc[2 * (i) + e].y); }
    u32 ia = INV_OFF;
    asm volatile("s_mov_b32 m0, %4\n\ts_waitcnt vmcnt(8)" : "+v"(xc[0]), "+v"(xc[1]), "+v"(xc[2]), "+v"(xc[3]) : "s"(U_BASE + 480 + 256 * ch));
    UNP(xa, 0, 0); UNP(xa, 1, 1);
    PIN(k0); PIN(k1); PIN(k2); PIN(ia);
    asm volatile("s_waitcnt vmcnt(4)" : "+v"(xc[4]), "+v"(xc[5]), "+v"(xc[6]), "+v"(xc[7]), "+{v[32:47]}"(xa));
    UNP(xb, 0, 2); UNP(xb, 1, 3);
    STAMP(2);
    asm volatile("s_waitcnt vmcnt(0)" : "+{v[64:67]}"(st[0]), "+{v[68:71]}"(st[1]), "+{v[72:75]}"(st[2]), "+{v[76:79]}"(st[3]), "+{v[48:63]}"(xb));
    STAMP(3);
    u32 cnt, lvl = 1 + (ch >> 1);
    u64 tm;
#define HST(k)
#define HOUT
    asm volatile(HP_ASM
                 : "+{v[64:67]}"(st[0]), "+{v[68:71]}"(st[1]), "+{v[72:75]}"(st[2]), "+{v[76:79]}"(st[3]), [o4] "=&s"(cnt), [o5] "=&s"(tm), [o6] "+s"(lvl) HOUT
                 : "{v[32:47]}"(xa), "{v[48:63]}"(xb), [o9] "v"(k0), [o10] "v"(k1), [o11] "v"(k2), [o12] "v"(fl), [o13] "v"(flw), [o14] "v"(go), [o15] "v"(k3),
                   [o16] "s"(tag), [o17] "s"(wr), [o18] "s"(out), [o19] "s"(out + 262144), [o20] "s"(out + 524288), [o21] "s"(out + 786432),
                   [o22] "n"(SPIN_CAP * 16), [o23] "v"(ia)
                 : "vcc", "scc", "memory", HP_CLOB);
  }
  STAMP_END(15);
  __builtin_amdgcn_s_waitcnt(0);
  __builtin_amdgcn_endpgm();
}

extern "C" void kernel_launch(void* const* d_in, const int* in_sizes, int n_in, void* d_out, int out_size, void* d_ws,
                              size_t ws_size, hipStream_t stream) {
  fused_kernel<<<256, 512, 0, stream>>>(
      (const u16*)d_in[0], (const u16*)d_in[1], (const u16*)d_in[2], (const u16*)d_in[3], (const u16*)d_in[4],
      (const u16*)d_in[5], (const u16*)d_in[6], (const u16*)d_in[7], (const u16*)d_in[8], (const u16*)d_in[10],
      (const u16*)d_in[11], (const u16*)d_in[12], (const u16*)d_in[13], (u16*)d_out, (char*)d_ws);
}
```
